```python
import torch

def _softmax(x, axis=-1):
    return torch.softmax(x, dim=axis)
def _gelu(x):
    return torch.nn.functional.gelu(x, approximate="tanh")
def _silu(x):
    return torch.nn.functional.silu(x)
def _sigmoid(x):
    return torch.sigmoid(x)
def _layernorm(x, axis=-1, eps=1e-5):
    m = x.mean(dim=axis, keepdim=True)
    v = x.var(dim=axis, keepdim=True, unbiased=False)
    return (x - m) / torch.sqrt(v + eps)
def _scatter_add(tgt, idx, src, axis=0):
    return tgt.clone().index_add_(axis, idx, src)
def _segment_sum(src, seg, n):
    out = torch.zeros((n,) + src.shape[1:], dtype=src.dtype)
    return out.index_add_(0, seg, src)
def _pad(x, pw):
    flat = tuple(v for pair in reversed(pw) for v in pair)
    return torch.nn.functional.pad(x, flat)

kernel_name = 'sequential_preLN__spectral_fnet__moe_arch_mod__prompt'

def setup_inputs():

    _w = (lambda a: (lambda t: t.to(torch.bfloat16) if t.is_floating_point() else t)(torch.as_tensor(a)))
    def _A_setup():
        B, S, D = 1, 2048, 640
        ns = torch.arange(S)[:, None] * torch.arange(S)[None, :]
        nd = torch.arange(D)[:, None] * torch.arange(D)[None, :]
        Ws_re = torch.cos(-2.0 * torch.pi * ns / S).to(float)
        Ws_im = torch.sin(-2.0 * torch.pi * ns / S).to(float)
        Wd_re = torch.cos(-2.0 * torch.pi * nd / D).to(float)
        Wd_im = torch.sin(-2.0 * torch.pi * nd / D).to(float)
        return {
            "x": _w((lambda *s: torch.randn(*s, dtype=torch.bfloat16) * 0.1)(B, S, D)),
            "dft_seq_re": _w(Ws_re),
            "dft_seq_im": _w(Ws_im),
            "dft_hid_re": _w(Wd_re),
            "dft_hid_im": _w(Wd_im),
        }
    _A_in = _A_setup()

    def _B_setup():
        B, S, D, F, C = 1, 2048, 640, 1536, 8
        return {
            "x": _w((lambda *s: torch.randn(*s, dtype=torch.bfloat16) * 0.1)(B, S, D)),
            "w_r": _w((lambda *s: torch.randn(*s, dtype=torch.bfloat16) * 1.6)(D)),
            "w_gate": _w((lambda *s: torch.randn(*s, dtype=torch.bfloat16) * 0.8)(D, F)),
            "w_up": _w((lambda *s: torch.randn(*s, dtype=torch.bfloat16) * 0.8)(D, F)),
            "w_down": _w((lambda *s: torch.randn(*s, dtype=torch.bfloat16) * 0.8)(F, D)),
        }
    _B_in = _B_setup()

    return {
        "A_x": _A_in["x"],
        "A_dft_seq_re": _A_in["dft_seq_re"],
        "A_dft_seq_im": _A_in["dft_seq_im"],
        "A_dft_hid_re": _A_in["dft_hid_re"],
        "A_dft_hid_im": _A_in["dft_hid_im"],
        "B_x": _B_in["x"],
        "B_w_r": _B_in["w_r"],
        "B_w_gate": _B_in["w_gate"],
        "B_w_up": _B_in["w_up"],
        "B_w_down": _B_in["w_down"],
    }

def reference(A_x, A_dft_seq_re, A_dft_seq_im, A_dft_hid_re, A_dft_hid_im, B_x, B_w_r, B_w_gate, B_w_up, B_w_down):
    def _A_ref(x, dft_seq_re, dft_seq_im, dft_hid_re, dft_hid_im):
        a = torch.einsum("bsd,ed->bse", x, dft_hid_re)
        b = torch.einsum("bsd,ed->bse", x, dft_hid_im)
        y = torch.einsum("ts,bse->bte", dft_seq_re, a) - torch.einsum("ts,bse->bte", dft_seq_im, b)
        return y

    def _B_ref(x, w_r, w_gate, w_up, w_down):
        B, S, D = x.shape
        C = 8
        r = x @ w_r
        idx = torch.argsort(-r, axis=-1)[:, :C]
        idx_e = (lambda x, n, axis: torch.repeat_interleave(x, n, dim=axis))(idx.reshape(B, C, 1), D, axis=2)
        x_sel = (lambda x, i, axis: torch.take_along_dim(x, i, dim=axis))(x, idx_e, axis=1)
        g = _silu(x_sel @ w_gate)
        u = x_sel @ w_up
        y = (g * u) @ w_down
        r_sel = _sigmoid((lambda x, i, axis: torch.take_along_dim(x, i, dim=axis))(r, idx, axis=1))
        y = y * r_sel.reshape(B, C, 1)
        b_off = (torch.arange(B) * S).reshape(B, 1)
        flat_idx = (idx + b_off).reshape(B * C)
        zero = (x * 0.0).reshape(B * S, D)
        upd = _scatter_add(zero, flat_idx, y.reshape(B * C, D), axis=0)
        return x + upd.reshape(B, S, D)

    x = A_x
    h = x + _A_ref(_layernorm(x, axis=-1), A_dft_seq_re, A_dft_seq_im, A_dft_hid_re, A_dft_hid_im)
    h2 = h + _B_ref(_layernorm(h, axis=-1), B_w_r, B_w_gate, B_w_up, B_w_down)
    return h2
```

```cpp
#include <hip/hip_runtime.h>
#include <hip/hip_bf16.h>

typedef unsigned short u16;
typedef __attribute__((ext_vector_type(8))) short s16x8;
typedef __attribute__((ext_vector_type(4))) float f32x4;
typedef __attribute__((ext_vector_type(4))) unsigned int u32x4;
typedef __attribute__((ext_vector_type(2))) unsigned int u32x2;

constexpr int S_ = 2048, D_ = 640, F_ = 1536, C_ = 8;

__device__ __forceinline__ float bf2f(u16 v) { return __uint_as_float(((unsigned)v) << 16); }
__device__ __forceinline__ u16 f2bf(float f) { __bf16 b = (__bf16)f; return __builtin_bit_cast(u16, b); }
__device__ __forceinline__ float rbf(float f) { return bf2f(f2bf(f)); }
__device__ __forceinline__ float lo_f(unsigned w) { return __uint_as_float(w << 16); }
__device__ __forceinline__ float hi_f(unsigned w) { return __uint_as_float(w & 0xffff0000u); }
__device__ __forceinline__ unsigned pack2(float a, float b) { return (unsigned)f2bf(a) | ((unsigned)f2bf(b) << 16); }

__device__ __forceinline__ float red16_sum(float v) {
    v += __shfl_xor(v, 1); v += __shfl_xor(v, 2); v += __shfl_xor(v, 4); v += __shfl_xor(v, 8);
    return v;
}

__device__ __forceinline__ void ln_finish(float sum_sq_dev, float mean, float& m_bf, float& rinv) {
    float v = rbf(sum_sq_dev * (1.0f / D_));
    float den = rbf(sqrtf(rbf(v + 1e-5f)));
    m_bf = rbf(mean);
    rinv = 1.0f / den;
}

constexpr int G1_PF = 10;

__global__ __launch_bounds__(256, 1) void k_gemm1(const u16* __restrict__ x, const u16* __restrict__ Wre,
                                                  const u16* __restrict__ Wim, u16* __restrict__ abT) {
    extern __shared__ __attribute__((aligned(16))) char smem[];
    const int bid = blockIdx.x, xcd = bid & 7, loc = bid >> 3;
    const int st = (xcd >> 1) * 8 + (loc & 7);
    const int et = (xcd & 1) * 4 + (loc >> 3);
    const int s0 = st * 64, e0 = et * 160;
    const u16* W = (et < 4) ? (Wre + (size_t)e0 * D_) : (Wim + (size_t)(e0 - D_) * D_);
    const int tid = threadIdx.x, w = tid >> 6, lane = tid & 63;
    const int r = lane & 15, q = lane >> 4;
    const int ws = w & 1, we = w >> 1;

    u32x4 xv[4][5];
#pragma unroll
    for (int i = 0; i < 4; ++i) {
        const u16* xr = x + (size_t)(s0 + w * 16 + i * 4 + q) * D_;
#pragma unroll
        for (int c = 0; c < 5; ++c) xv[i][c] = *reinterpret_cast<const u32x4*>(xr + (c * 16 + r) * 8);
    }
    const u16* wbase = W + (size_t)(we * 80 + r) * D_ + q * 8;
    s16x8 wf[20][5];
#pragma unroll
    for (int ks = 0; ks < G1_PF; ++ks)
#pragma unroll
        for (int n = 0; n < 5; ++n)
            wf[ks][n] = *reinterpret_cast<const s16x8*>(wbase + (size_t)n * 16 * D_ + ks * 32);

#pragma unroll
    for (int i = 0; i < 4; ++i) {
        float sum = 0.f;
#pragma unroll
        for (int c = 0; c < 5; ++c)
#pragma unroll
            for (int j = 0; j < 4; ++j) sum += lo_f(xv[i][c][j]) + hi_f(xv[i][c][j]);
        sum = red16_sum(sum);
        const float mean = sum * (1.0f / D_);
        float sq = 0.f;
#pragma unroll
        for (int c = 0; c < 5; ++c)
#pragma unroll
            for (int j = 0; j < 4; ++j) {
                float a = lo_f(xv[i][c][j]) - mean, b = hi_f(xv[i][c][j]) - mean;
                sq += a * a + b * b;
            }
        sq = red16_sum(sq);
        float m_bf, rinv;
        ln_finish(sq, mean, m_bf, rinv);
        const int row = w * 16 + i * 4 + q;
#pragma unroll
        for (int c = 0; c < 5; ++c) {
            u32x4 o;
#pragma unroll
            for (int j = 0; j < 4; ++j) {
                float a = rbf(lo_f(xv[i][c][j]) - m_bf) * rinv;
                float b = rbf(hi_f(xv[i][c][j]) - m_bf) * rinv;
                o[j] = pack2(a, b);
            }
            const int ch = (c * 16 + r) ^ (row & 15);
            *reinterpret_cast<u32x4*>(smem + row * 1280 + ch * 16) = o;
        }
    }
    __syncthreads();

    f32x4 acc[2][5];
#pragma unroll
    for (int m = 0; m < 2; ++m)
#pragma unroll
        for (int n = 0; n < 5; ++n) acc[m][n] = f32x4{0.f, 0.f, 0.f, 0.f};

#pragma unroll
    for (int ks = 0; ks < 20; ++ks) {
        s16x8 xf[2];
#pragma unroll
        for (int m = 0; m < 2; ++m) {
            const int row = ws * 32 + m * 16 + r;
            const int ch = (ks * 4 + q) ^ (row & 15);
            xf[m] = *reinterpret_cast<const s16x8*>(smem + row * 1280 + ch * 16);
        }
        if (ks + G1_PF < 20) {
#pragma unroll
            for (int n = 0; n < 5; ++n)
                wf[ks + G1_PF][n] = *reinterpret_cast<const s16x8*>(wbase + (size_t)n * 16 * D_ + (ks + G1_PF) * 32);
        }
#pragma unroll
        for (int m = 0; m < 2; ++m)
#pragma unroll
            for (int n = 0; n < 5; ++n)
                acc[m][n] = __builtin_amdgcn_mfma_f32_16x16x32_bf16(xf[m], wf[ks][n], acc[m][n], 0, 0, 0);
    }
#pragma unroll
    for (int m = 0; m < 2; ++m)
#pragma unroll
        for (int n = 0; n < 5; ++n) {
            const int e = e0 + we * 80 + n * 16 + r;
            const int s = s0 + ws * 32 + m * 16 + q * 4;
            u32x2 o;
            o[0] = pack2(acc[m][n][0], acc[m][n][1]);
            o[1] = pack2(acc[m][n][2], acc[m][n][3]);
            *reinterpret_cast<u32x2*>(abT + (size_t)e * S_ + s) = o;
        }
}

constexpr int G2_SLOT = 80 * 128;

__device__ __forceinline__ void g2_load_c(s16x8 (&cf)[4], const u16* base, unsigned v0, unsigned v1) {
    asm volatile("global_load_dwordx4 %0, %1, %2" : "=v"(cf[0]) : "v"(v0), "s"(base) : "memory");
    asm volatile("global_load_dwordx4 %0, %1, %2 offset:64" : "=v"(cf[1]) : "v"(v0), "s"(base) : "memory");
    asm volatile("global_load_dwordx4 %0, %1, %2" : "=v"(cf[2]) : "v"(v1), "s"(base) : "memory");
    asm volatile("global_load_dwordx4 %0, %1, %2 offset:64" : "=v"(cf[3]) : "v"(v1), "s"(base) : "memory");
}
__device__ __forceinline__ void g2_dma(const u16* base, unsigned voff, unsigned ldsaddr) {
    unsigned keep;
    asm volatile("s_mov_b32 %0, m0\n\ts_mov_b32 m0, %3\n\ts_nop 0\n\tglobal_load_lds_dwordx4 %1, %2\n\ts_mov_b32 m0, %0"
                 : "=&s"(keep) : "v"(voff), "s"(base), "s"(ldsaddr) : "memory");
}

__global__ __launch_bounds__(256, 1) void k_gemm2(const u16* __restrict__ Cre, const u16* __restrict__ Cim,
                                                  const u16* __restrict__ abT, u16* __restrict__ P) {
    extern __shared__ __attribute__((aligned(16))) char smem[];
    const int bid = blockIdx.x, xcd = bid & 7, loc = bid >> 3;
    const int part = xcd & 1;
    const int tp = (xcd >> 1) * 4 + (loc >> 3);
    const int ct = loc & 7;
    const int t0 = tp * 128, e0 = ct * 80;
    const int tid = threadIdx.x, lane = tid & 63;
    const int w = __builtin_amdgcn_readfirstlane(tid >> 6);
    const int r = lane & 15, q = lane >> 4;

    const u16* Cb = (part ? Cim : Cre) + (size_t)(t0 + w * 32) * S_;
    const u16* Bb = abT + (size_t)(part * D_ + e0) * S_;
    const unsigned cv0 = (unsigned)(r * S_ * 2 + q * 16);
    const unsigned cv1 = cv0 + 16u * S_ * 2;
    unsigned bv[3];
#pragma unroll
    for (int i = 0; i < 3; ++i) {
        const int o = w * 2560 + i * 1024 + lane * 16;
        const int row = o >> 7, cs = (o >> 4) & 7;
        bv[i] = (unsigned)(row * S_ * 2 + ((cs ^ (row & 7)) * 16));
    }
    const unsigned lds0 = (unsigned)(size_t)0 + (unsigned)(w * 2560);
    const int rd0 = r * 128 + ((q ^ (r & 7)) * 16);
    const int rd1 = r * 128 + (((q + 4) ^ (r & 7)) * 16);

    s16x8 cf[8][4];
    s16x8 ef[2][10];
    f32x4 acc[5][2];
#pragma unroll
    for (int n = 0; n < 5; ++n)
#pragma unroll
        for (int m = 0; m < 2; ++m) acc[n][m] = f32x4{0.f, 0.f, 0.f, 0.f};

    const unsigned smem_base = (unsigned)(size_t)(smem);

#define G2_ISSUE(SET, KSTEP)                                                          \
    do {                                                                              \
        const int kk_ = (KSTEP) < 31 ? (KSTEP) : 31;                                  \
        const u16* cb_ = Cb + kk_ * 64;                                               \
        const u16* bb_ = Bb + kk_ * 64;                                               \
        const unsigned la_ = smem_base + lds0 + (unsigned)((SET) * G2_SLOT);          \
        g2_load_c(cf[SET], cb_, cv0, cv1);                                            \
        g2_dma(bb_, bv[0], la_);                                                      \
        g2_dma(bb_, bv[1], la_ + 1024u);                                              \
        if (lane < 32) g2_dma(bb_, bv[2], la_ + 2048u);                               \
    } while (0)
#define G2_READ_E(DST, SET)                                                           \
    do {                                                                              \
        _Pragma("unroll") for (int n_ = 0; n_ < 5; ++n_) {                            \
            DST[n_ * 2 + 0] = *reinterpret_cast<const s16x8*>(smem + (SET) * G2_SLOT + n_ * 2048 + rd0); \
            DST[n_ * 2 + 1] = *reinterpret_cast<const s16x8*>(smem + (SET) * G2_SLOT + n_ * 2048 + rd1); \
        }                                                                             \
    } while (0)
#define G2_WAIT_BAR(SET)                                                              \
    asm volatile("s_waitcnt vmcnt(35) lgkmcnt(0)\n\ts_barrier"                        \
                 : "+v"(cf[SET][0]), "+v"(cf[SET][1]), "+v"(cf[SET][2]), "+v"(cf[SET][3]) :: "memory")
#define G2_MMA(EF, SET)                                                               \
    do {                                                                              \
        _Pragma("unroll") for (int h_ = 0; h_ < 2; ++h_)                              \
        _Pragma("unroll") for (int n_ = 0; n_ < 5; ++n_)                              \
        _Pragma("unroll") for (int m_ = 0; m_ < 2; ++m_)                              \
            acc[n_][m_] = __builtin_amdgcn_mfma_f32_16x16x32_bf16(EF[n_ * 2 + h_], cf[SET][m_ * 2 + h_], acc[n_][m_], 0, 0, 0); \
    } while (0)

    G2_ISSUE(0, 0); G2_ISSUE(1, 1); G2_ISSUE(2, 2); G2_ISSUE(3, 3);
    G2_ISSUE(4, 4); G2_ISSUE(5, 5); G2_ISSUE(6, 6);
    asm volatile("s_waitcnt vmcnt(42)\n\ts_barrier"
                 : "+v"(cf[0][0]), "+v"(cf[0][1]), "+v"(cf[0][2]), "+v"(cf[0][3]) :: "memory");
    G2_READ_E(ef[0], 0);

    for (int kb = 0; kb < 32; kb += 8) {
#define G2_STEP(J)                                                                    \
        G2_WAIT_BAR((J + 1) & 7);                                                     \
        G2_ISSUE((J + 7) & 7, kb + J + 7);                                            \
        G2_READ_E(ef[(J + 1) & 1], (J + 1) & 7);                                      \
        G2_MMA(ef[J & 1], J);
        G2_STEP(0) G2_STEP(1) G2_STEP(2) G2_STEP(3)
        G2_STEP(4) G2_STEP(5) G2_STEP(6) G2_STEP(7)
#undef G2_STEP
    }
    asm volatile("s_waitcnt vmcnt(0) lgkmcnt(0)" ::: "memory");

    u16* Pp = P + (size_t)part * S_ * D_;
#pragma unroll
    for (int m = 0; m < 2; ++m)
#pragma unroll
        for (int n = 0; n < 5; ++n) {
            const int t = t0 + w * 32 + m * 16 + r;
            const int e = e0 + n * 16 + q * 4;
            u32x2 o;
            o[0] = pack2(acc[n][m][0], acc[n][m][1]);
            o[1] = pack2(acc[n][m][2], acc[n][m][3]);
            *reinterpret_cast<u32x2*>(Pp + (size_t)t * D_ + e) = o;
        }
}

__global__ __launch_bounds__(64) void k_ln2(const u16* __restrict__ x, const u16* __restrict__ P,
                                            const u16* __restrict__ wr, u16* __restrict__ out,
                                            float* __restrict__ rbuf, float2* __restrict__ stats) {
    const int lane = threadIdx.x & 63, r = lane & 15, q = lane >> 4;
    const int row = blockIdx.x * 4 + q;
    const size_t ro = (size_t)row * D_;
    u32x4 xv[5], p1[5], p2[5], wv[5];
#pragma unroll
    for (int c = 0; c < 5; ++c) {
        const int o = (c * 16 + r) * 8;
        xv[c] = *reinterpret_cast<const u32x4*>(x + ro + o);
        p1[c] = *reinterpret_cast<const u32x4*>(P + ro + o);
        p2[c] = *reinterpret_cast<const u32x4*>(P + (size_t)S_ * D_ + ro + o);
        wv[c] = *reinterpret_cast<const u32x4*>(wr + o);
    }
    float h[40];
    float sum = 0.f;
#pragma unroll
    for (int c = 0; c < 5; ++c)
#pragma unroll
        for (int j = 0; j < 4; ++j) {
            float a = rbf(lo_f(xv[c][j]) + rbf(lo_f(p1[c][j]) - lo_f(p2[c][j])));
            float b = rbf(hi_f(xv[c][j]) + rbf(hi_f(p1[c][j]) - hi_f(p2[c][j])));
            h[c * 8 + j * 2] = a; h[c * 8 + j * 2 + 1] = b;
            sum += a + b;
        }
    sum = red16_sum(sum);
    const float mean = sum * (1.0f / D_);
    float sq = 0.f;
#pragma unroll
    for (int i = 0; i < 40; ++i) { float d = h[i] - mean; sq += d * d; }
    sq = red16_sum(sq);
    float m_bf, rinv;
    ln_finish(sq, mean, m_bf, rinv);
    float dot = 0.f;
#pragma unroll
    for (int c = 0; c < 5; ++c) {
        u32x4 o;
#pragma unroll
        for (int j = 0; j < 4; ++j) {
            float ha = h[c * 8 + j * 2], hb = h[c * 8 + j * 2 + 1];
            float a = rbf(rbf(ha - m_bf) * rinv);
            float b = rbf(rbf(hb - m_bf) * rinv);
            dot += a * lo_f(wv[c][j]) + b * hi_f(wv[c][j]);
            o[j] = pack2(ha + a, hb + b);
        }
        *reinterpret_cast<u32x4*>(out + ro + (c * 16 + r) * 8) = o;
    }
    dot = red16_sum(dot);
    if (r == 0) {
        rbuf[row] = rbf(dot);
        stats[row] = make_float2(m_bf, rinv);
    }
}

__device__ __forceinline__ unsigned topk_key(float v, int idx) {
    unsigned b = __float_as_uint(v) >> 16;
    b = (b & 0x8000u) ? (~b & 0xffffu) : (b | 0x8000u);
    return (b << 16) | (unsigned)(S_ - 1 - idx);
}
__device__ __forceinline__ unsigned wave_max_u32(unsigned v) {
#pragma unroll
    for (int o = 1; o < 64; o <<= 1) { unsigned t = __shfl_xor(v, o); v = v > t ? v : t; }
    return v;
}

__device__ __forceinline__ void row_chunk(const u16* x, const u16* P, int row, int col, float2 st,
                                          float (&h)[8], float (&ln)[8]) {
    const size_t o = (size_t)row * D_ + col;
    u32x4 xv = *reinterpret_cast<const u32x4*>(x + o);
    u32x4 p1 = *reinterpret_cast<const u32x4*>(P + o);
    u32x4 p2 = *reinterpret_cast<const u32x4*>(P + (size_t)S_ * D_ + o);
#pragma unroll
    for (int j = 0; j < 4; ++j) {
        h[2 * j] = rbf(lo_f(xv[j]) + rbf(lo_f(p1[j]) - lo_f(p2[j])));
        h[2 * j + 1] = rbf(hi_f(xv[j]) + rbf(hi_f(p1[j]) - hi_f(p2[j])));
    }
#pragma unroll
    for (int j = 0; j < 8; ++j) ln[j] = rbf(rbf(h[j] - st.x) * st.y);
}

__global__ __launch_bounds__(256) void k_ffn_up(const u16* __restrict__ x, const u16* __restrict__ P,
                                                const float* __restrict__ rbuf, const float2* __restrict__ stats,
                                                const u16* __restrict__ wg, const u16* __restrict__ wu,
                                                u16* __restrict__ act, int* __restrict__ sel_idx,
                                                float* __restrict__ sel_sig) {
    __shared__ float red[4][2][8][16];
    const int bid = blockIdx.x;
    const int cb = (bid & 7) * 12 + (bid >> 3);
    const int c0 = cb * 16;
    const int tid = threadIdx.x, w = tid >> 6, lane = tid & 63, r = lane & 15, q = lane >> 4;

    s16x8 gf[5], uf[5];
#pragma unroll
    for (int i = 0; i < 5; ++i) {
        const size_t kb = (size_t)((w * 5 + i) * 32 + q * 8) * F_ + c0 + r;
#pragma unroll
        for (int j = 0; j < 8; ++j) {
            gf[i][j] = (short)wg[kb + (size_t)j * F_];
            uf[i][j] = (short)wu[kb + (size_t)j * F_];
        }
    }
    unsigned key[32];
#pragma unroll
    for (int i = 0; i < 8; ++i) {
        const int idx = (i * 64 + lane) * 4;
        const float4 v = *reinterpret_cast<const float4*>(rbuf + idx);
        key[i * 4 + 0] = topk_key(v.x, idx + 0); key[i * 4 + 1] = topk_key(v.y, idx + 1);
        key[i * 4 + 2] = topk_key(v.z, idx + 2); key[i * 4 + 3] = topk_key(v.w, idx + 3);
    }
    int my_row = 0;
    unsigned my_key = 0;
    unsigned lm = 0;
#pragma unroll
    for (int i = 0; i < 32; ++i) lm = lm > key[i] ? lm : key[i];
    for (int t = 0; t < 8; ++t) {
        const unsigned g = wave_max_u32(lm);
        if (r == t) { my_row = (int)(S_ - 1 - (g & 0xffffu)); my_key = g; }
        unsigned nm = 0;
#pragma unroll
        for (int i = 0; i < 32; ++i) { key[i] = (key[i] == g) ? 0u : key[i]; nm = nm > key[i] ? nm : key[i]; }
        lm = nm;
    }
    (void)my_key;
    s16x8 af[5];
    if (r < 8) {
        const float2 st = stats[my_row];
#pragma unroll
        for (int i = 0; i < 5; ++i) {
            float h[8], ln[8];
            row_chunk(x, P, my_row, (w * 5 + i) * 32 + q * 8, st, h, ln);
#pragma unroll
            for (int j = 0; j < 8; ++j) af[i][j] = (short)f2bf(ln[j]);
        }
    } else {
#pragma unroll
        for (int i = 0; i < 5; ++i) af[i] = s16x8{0, 0, 0, 0, 0, 0, 0, 0};
    }
    if (bid == 0 && w == 0 && q == 0 && r < 8) {
        const float rv = rbuf[my_row];
        sel_idx[r] = my_row;
        sel_sig[r] = rbf(1.0f / (1.0f + __expf(-rv)));
    }
    f32x4 ag = {0.f, 0.f, 0.f, 0.f}, au = {0.f, 0.f, 0.f, 0.f};
#pragma unroll
    for (int i = 0; i < 5; ++i) {
        ag = __builtin_amdgcn_mfma_f32_16x16x32_bf16(af[i], gf[i], ag, 0, 0, 0);
        au = __builtin_amdgcn_mfma_f32_16x16x32_bf16(af[i], uf[i], au, 0, 0, 0);
    }
    if (q < 2) {
#pragma unroll
        for (int j = 0; j < 4; ++j) { red[w][0][q * 4 + j][r] = ag[j]; red[w][1][q * 4 + j][r] = au[j]; }
    }
    __syncthreads();
    if (tid < 128) {
        const int t = tid >> 4, c = tid & 15;
        float g = red[0][0][t][c] + red[1][0][t][c] + red[2][0][t][c] + red[3][0][t][c];
        float u = red[0][1][t][c] + red[1][1][t][c] + red[2][1][t][c] + red[3][1][t][c];
        g = rbf(g); u = rbf(u);
        const float sg = rbf(g / (1.0f + __expf(-g)));
        act[t * F_ + c0 + c] = f2bf(sg * u);
    }
}

__global__ __launch_bounds__(512) void k_ffn_dn(const u16* __restrict__ x, const u16* __restrict__ P,
                                                const float2* __restrict__ stats, const u16* __restrict__ wd,
                                                const u16* __restrict__ act, const int* __restrict__ sel_idx,
                                                const float* __restrict__ sel_sig, u16* __restrict__ out) {
    __shared__ float red[8][8][16];
    const int bid = blockIdx.x;
    const int cb = (bid & 7) * 5 + (bid >> 3);
    const int c0 = cb * 16;
    const int tid = threadIdx.x, w = tid >> 6, lane = tid & 63, r = lane & 15, q = lane >> 4;
    s16x8 wf[6], af[6];
#pragma unroll
    for (int i = 0; i < 6; ++i) {
        const size_t kb = (size_t)((w * 6 + i) * 32 + q * 8) * D_ + c0 + r;
#pragma unroll
        for (int j = 0; j < 8; ++j) wf[i][j] = (short)wd[kb + (size_t)j * D_];
    }
#pragma unroll
    for (int i = 0; i < 6; ++i) {
        if (r < 8) af[i] = *reinterpret_cast<const s16x8*>(act + r * F_ + (w * 6 + i) * 32 + q * 8);
        else af[i] = s16x8{0, 0, 0, 0, 0, 0, 0, 0};
    }
    f32x4 a = {0.f, 0.f, 0.f, 0.f};
#pragma unroll
    for (int i = 0; i < 6; ++i) a = __builtin_amdgcn_mfma_f32_16x16x32_bf16(af[i], wf[i], a, 0, 0, 0);
    if (q < 2) {
#pragma unroll
        for (int j = 0; j < 4; ++j) red[w][q * 4 + j][r] = a[j];
    }
    __syncthreads();
    if (tid < 128) {
        const int t = tid >> 4, c = tid & 15;
        float y = 0.f;
#pragma unroll
        for (int i = 0; i < 8; ++i) y += red[i][t][c];
        y = rbf(y);
        y = rbf(y * sel_sig[t]);
        const int row = sel_idx[t];
        const float2 st = stats[row];
        const size_t o = (size_t)row * D_ + c0 + c;
        const float hv = rbf(bf2f(x[o]) + rbf(bf2f(P[o]) - bf2f(P[(size_t)S_ * D_ + o])));
        const float ln = rbf(rbf(hv - st.x) * st.y);
        out[o] = f2bf(hv + rbf(ln + y));
    }
}

extern "C" void kernel_launch(void* const* d_in, const int* in_sizes, int n_in,
                              void* d_out, int out_size, void* d_ws, size_t ws_size,
                              hipStream_t stream) {
    const u16* x   = (const u16*)d_in[0];
    const u16* Cre = (const u16*)d_in[1];
    const u16* Cim = (const u16*)d_in[2];
    const u16* Wre = (const u16*)d_in[3];
    const u16* Wim = (const u16*)d_in[4];
    const u16* wr  = (const u16*)d_in[6];
    const u16* wg  = (const u16*)d_in[7];
    const u16* wu  = (const u16*)d_in[8];
    const u16* wd  = (const u16*)d_in[9];
    u16* out = (u16*)d_out;

    char* ws = (char*)d_ws;
    u16* abT = (u16*)ws;
    u16* P = (u16*)(ws + (size_t)2 * D_ * S_ * 2);
    char* tail = ws + (size_t)4 * D_ * S_ * 2;
    float* rbuf = (float*)tail;
    float2* stats = (float2*)(tail + 8192);
    u16* act = (u16*)(tail + 8192 + 16384);
    int* sel_idx = (int*)(tail + 8192 + 16384 + 24576);
    float* sel_sig = (float*)(tail + 8192 + 16384 + 24576 + 64);

    hipFuncSetAttribute((const void*)k_gemm1, hipFuncAttributeMaxDynamicSharedMemorySize, 64 * 1280);
    hipFuncSetAttribute((const void*)k_gemm2, hipFuncAttributeMaxDynamicSharedMemorySize, 8 * G2_SLOT);

    k_gemm1<<<256, 256, 64 * 1280, stream>>>(x, Wre, Wim, abT);
    k_gemm2<<<256, 256, 8 * G2_SLOT, stream>>>(Cre, Cim, abT, P);
    k_ln2<<<512, 64, 0, stream>>>(x, P, wr, out, rbuf, stats);
    k_ffn_up<<<96, 256, 0, stream>>>(x, P, rbuf, stats, wg, wu, act, sel_idx, sel_sig);
    k_ffn_dn<<<40, 512, 0, stream>>>(x, P, stats, wd, act, sel_idx, sel_sig, out);
}
```

```cpp
#include <hip/hip_runtime.h>
#include <hip/hip_bf16.h>

typedef unsigned short u16;
typedef __attribute__((ext_vector_type(8))) short s16x8;
typedef __attribute__((ext_vector_type(4))) float f32x4;
typedef __attribute__((ext_vector_type(4))) unsigned int u32x4;
typedef __attribute__((ext_vector_type(2))) unsigned int u32x2;

constexpr int S_ = 2048, D_ = 640, F_ = 1536, C_ = 8;

__device__ __forceinline__ float bf2f(u16 v) { return __uint_as_float(((unsigned)v) << 16); }
__device__ __forceinline__ u16 f2bf(float f) { __bf16 b = (__bf16)f; return __builtin_bit_cast(u16, b); }
__device__ __forceinline__ float rbf(float f) { return bf2f(f2bf(f)); }
__device__ __forceinline__ float lo_f(unsigned w) { return __uint_as_float(w << 16); }
__device__ __forceinline__ float hi_f(unsigned w) { return __uint_as_float(w & 0xffff0000u); }
__device__ __forceinline__ unsigned pack2(float a, float b) { return (unsigned)f2bf(a) | ((unsigned)f2bf(b) << 16); }

template <int CTRL> __device__ __forceinline__ unsigned dpp_u(unsigned v) {
    return (unsigned)__builtin_amdgcn_update_dpp(0, (int)v, CTRL, 0xf, 0xf, false);
}
template <int CTRL> __device__ __forceinline__ float dpp_f(float v) { return __uint_as_float(dpp_u<CTRL>(__float_as_uint(v))); }
__device__ __forceinline__ float red16_sum(float v) {
    v += dpp_f<0x128>(v); v += dpp_f<0x124>(v); v += dpp_f<0x122>(v); v += dpp_f<0x121>(v);
    return v;
}

__device__ __forceinline__ void ln_finish(float sum_sq_dev, float mean, float& m_bf, float& rinv) {
    float v = rbf(sum_sq_dev * (1.0f / D_));
    float den = rbf(sqrtf(rbf(v + 1e-5f)));
    m_bf = rbf(mean);
    rinv = 1.0f / den;
}

__device__ __forceinline__ void dma16(const u16* base, unsigned voff, unsigned ldsaddr) {
    unsigned keep;
    asm volatile("s_mov_b32 %0, m0\n\ts_mov_b32 m0, %3\n\ts_nop 0\n\tglobal_load_lds_dwordx4 %1, %2\n\ts_mov_b32 m0, %0"
                 : "=&s"(keep) : "v"(voff), "s"(base), "s"(ldsaddr) : "memory");
}
__device__ __forceinline__ unsigned l2_touch(const u16* base, unsigned voff) {
    unsigned d;
    asm volatile("global_load_dword %0, %1, %2" : "=v"(d) : "v"(voff), "s"(base) : "memory");
    return d;
}
#define l2_touch_done5(a, b, c, d, e) asm volatile("s_waitcnt vmcnt(0)" : "+v"(a), "+v"(b), "+v"(c), "+v"(d), "+v"(e) :: "memory")
#define l2_touch_done1(a) asm volatile("s_waitcnt vmcnt(0)" : "+v"(a) :: "memory")
#define LGKM_BAR() asm volatile("s_waitcnt lgkmcnt(0)\n\ts_barrier" ::: "memory")
#define VM_BAR(N) asm volatile("s_waitcnt vmcnt(" #N ")\n\ts_barrier" ::: "memory")
#define FRAG_BAR(A, B)                                                                              \
    asm volatile("s_waitcnt lgkmcnt(0)\n\ts_barrier"                                                \
                 : "+v"(A[0]), "+v"(A[1]), "+v"(A[2]), "+v"(A[3]), "+v"(B[0]), "+v"(B[1]), "+v"(B[2]), \
                   "+v"(B[3]), "+v"(B[4]), "+v"(B[5]), "+v"(B[6]), "+v"(B[7]), "+v"(B[8]), "+v"(B[9]) \
                 :: "memory")
#define INTERLEAVE_14_20()                                                              \
    do {                                                                                \
        _Pragma("unroll") for (int i_ = 0; i_ < 14; ++i_) {                             \
            __builtin_amdgcn_sched_group_barrier(0x100, 1, 0);                          \
            __builtin_amdgcn_sched_group_barrier(0x008, 1, 0);                          \
        }                                                                               \
        __builtin_amdgcn_sched_group_barrier(0x008, 6, 0);                              \
        __builtin_amdgcn_sched_barrier(0);                                              \
    } while (0)

constexpr int G1_XN = 64 * 1280;
constexpr int G1_SLOT = 160 * 128;
constexpr int G1_LDS = G1_XN + 4 * G1_SLOT;

__global__ __launch_bounds__(512, 2) void k_gemm1(const u16* __restrict__ x, const u16* __restrict__ Wre,
                                                  const u16* __restrict__ Wim, u16* __restrict__ abT,
                                                  unsigned* __restrict__ cnt) {
    extern __shared__ __attribute__((aligned(16))) char smem[];
    const int bid = blockIdx.x, xcd = bid & 7, loc = bid >> 3;
    if (bid == 0 && threadIdx.x < 16) cnt[threadIdx.x * 64] = 0u;
    const int st = (xcd >> 1) * 8 + (loc & 7);
    const int et = (xcd & 1) * 4 + (loc >> 3);
    const int s0 = st * 64, e0 = et * 160;
    const u16* W = (et < 4) ? (Wre + (size_t)e0 * D_) : (Wim + (size_t)(e0 - D_) * D_);
    const int tid = threadIdx.x, lane = tid & 63;
    const int w = __builtin_amdgcn_readfirstlane(tid >> 6);
    const int r = lane & 15, q = lane >> 4;
    const unsigned smem_base = (unsigned)(size_t)(smem);

    u32x4 xv[2][5];
#pragma unroll
    for (int i = 0; i < 2; ++i) {
        const u16* xr = x + (size_t)(s0 + w * 8 + i * 4 + q) * D_;
#pragma unroll
        for (int c = 0; c < 5; ++c) xv[i][c] = *reinterpret_cast<const u32x4*>(xr + (c * 16 + r) * 8);
    }
    unsigned wv[5];
    const int p = w - 4;
#pragma unroll
    for (int i = 0; i < 5; ++i) {
        const int row = (p & 3) * 40 + i * 8 + (lane >> 3);
        wv[i] = (unsigned)(row * D_ * 2 + (((lane & 7) ^ (row & 7)) * 16));
    }
    const unsigned wl = smem_base + G1_XN + (unsigned)((p & 3) * 5120);
#define G1_ISSUE(K)                                                                     \
    do {                                                                                \
        _Pragma("unroll") for (int i_ = 0; i_ < 5; ++i_)                                \
            dma16(W + (K) * 64, wv[i_], wl + (unsigned)(((K) & 3) * G1_SLOT + i_ * 1024)); \
    } while (0)
    if (w >= 4) { G1_ISSUE(0); G1_ISSUE(1); G1_ISSUE(2); G1_ISSUE(3); }
    unsigned pf = 0;
    if (w < 4) {
        int li = w * 64 + lane;
        li = li < 200 ? li : 199;
        pf = l2_touch(W + (size_t)((loc & 7) * 20) * D_, (unsigned)((li % 20) * D_ * 2 + (li / 20) * 128));
    }

#pragma unroll
    for (int i = 0; i < 2; ++i) {
        float sum = 0.f;
#pragma unroll
        for (int c = 0; c < 5; ++c)
#pragma unroll
            for (int j = 0; j < 4; ++j) sum += lo_f(xv[i][c][j]) + hi_f(xv[i][c][j]);
        sum = red16_sum(sum);
        const float mean = sum * (1.0f / D_);
        float sq = 0.f;
#pragma unroll
        for (int c = 0; c < 5; ++c)
#pragma unroll
            for (int j = 0; j < 4; ++j) {
                float a = lo_f(xv[i][c][j]) - mean, b = hi_f(xv[i][c][j]) - mean;
                sq += a * a + b * b;
            }
        sq = red16_sum(sq);
        float m_bf, rinv;
        ln_finish(sq, mean, m_bf, rinv);
        const int row = w * 8 + i * 4 + q;
#pragma unroll
        for (int c = 0; c < 5; ++c) {
            u32x4 o;
#pragma unroll
            for (int j = 0; j < 4; ++j) {
                float a = rbf(lo_f(xv[i][c][j]) - m_bf) * rinv;
                float b = rbf(hi_f(xv[i][c][j]) - m_bf) * rinv;
                o[j] = pack2(a, b);
            }
            const int ch = (c * 16 + r) ^ (row & 15);
            *reinterpret_cast<u32x4*>(smem + row * 1280 + ch * 16) = o;
        }
    }

    if (w >= 4) {
        asm volatile("s_waitcnt vmcnt(15) lgkmcnt(0)\n\ts_barrier" ::: "memory");
#pragma unroll
        for (int k = 0; k < 10; ++k) {
            if (k <= 6) VM_BAR(10); else VM_BAR(0);
            if (k + 4 < 10) G1_ISSUE(k + 4);
        }
        return;
    }
    l2_touch_done1(pf);
    const int ws = w & 1, we = w >> 1;
    f32x4 acc[2][5];
#pragma unroll
    for (int m = 0; m < 2; ++m)
#pragma unroll
        for (int n = 0; n < 5; ++n) acc[m][n] = f32x4{0.f, 0.f, 0.f, 0.f};
    const char* xbase = smem + (ws * 32 + r) * 1280;
    const char* wbase = smem + G1_XN + (we * 80 + r) * 128;
    s16x8 xf[2][4], wf[2][10];
#define G1_READ(B, K)                                                                   \
    do {                                                                                \
        _Pragma("unroll") for (int h_ = 0; h_ < 2; ++h_) {                              \
            _Pragma("unroll") for (int m_ = 0; m_ < 2; ++m_)                            \
                xf[B][m_ * 2 + h_] = *reinterpret_cast<const s16x8*>(xbase + m_ * 16 * 1280 + ((((K) * 8 + h_ * 4 + q) ^ r) * 16)); \
            _Pragma("unroll") for (int n_ = 0; n_ < 5; ++n_)                            \
                wf[B][n_ * 2 + h_] = *reinterpret_cast<const s16x8*>(wbase + ((K) & 3) * G1_SLOT + n_ * 2048 + (((h_ * 4 + q) ^ (r & 7)) * 16)); \
        }                                                                               \
    } while (0)
    LGKM_BAR();
    G1_READ(0, 0);
#pragma unroll
    for (int k = 0; k < 10; ++k) {
        FRAG_BAR(xf[k & 1], wf[k & 1]);
        if (k + 1 < 10) G1_READ((k + 1) & 1, k + 1);
#pragma unroll
        for (int h = 0; h < 2; ++h)
#pragma unroll
            for (int m = 0; m < 2; ++m)
#pragma unroll
                for (int n = 0; n < 5; ++n)
                    acc[m][n] = __builtin_amdgcn_mfma_f32_16x16x32_bf16(xf[k & 1][m * 2 + h], wf[k & 1][n * 2 + h], acc[m][n], 0, 0, 0);
        if (k + 1 < 10) INTERLEAVE_14_20();
    }
#pragma unroll
    for (int m = 0; m < 2; ++m)
#pragma unroll
        for (int n = 0; n < 5; ++n) {
            const int e = e0 + we * 80 + n * 16 + r;
            const int s = s0 + ws * 32 + m * 16 + q * 4;
            u32x2 o;
            o[0] = pack2(acc[m][n][0], acc[m][n][1]);
            o[1] = pack2(acc[m][n][2], acc[m][n][3]);
            *reinterpret_cast<u32x2*>(abT + (size_t)e * S_ + s) = o;
        }
}

#define G2_ROT 0
#define G2_T2 8
#define G2_T3 16
constexpr int G2_SLOT = (128 + 80) * 128;
constexpr int G2_NST = 6;
constexpr int G2_LDS = G2_NST * G2_SLOT;

__global__ __launch_bounds__(512, 2) void k_gemm2(const u16* __restrict__ Cre, const u16* __restrict__ Cim,
                                                  const u16* __restrict__ abT, u16* __restrict__ P) {
    extern __shared__ __attribute__((aligned(16))) char smem[];
    const int bid = blockIdx.x, xcd = bid & 7, loc = bid >> 3;
    const int part = xcd & 1;
    const int tp = (xcd >> 1) * 4 + (loc >> 3);
    const int ct = loc & 7;
    const int t0 = tp * 128, e0 = ct * 80;
    const int tid = threadIdx.x, lane = tid & 63;
    const int w = __builtin_amdgcn_readfirstlane(tid >> 6);
    const int r = lane & 15, q = lane >> 4;
    const unsigned smem_base = (unsigned)(size_t)(smem);

    if (w >= 4) {
        const int p = w - 4;
        const u16* Cb = (part ? Cim : Cre) + (size_t)t0 * S_;
        const u16* Bb = abT + (size_t)(part * D_ + e0) * S_;
        unsigned av[4], bv[3];
#pragma unroll
        for (int i = 0; i < 4; ++i) {
            const int row = p * 32 + i * 8 + (lane >> 3);
            av[i] = (unsigned)(row * S_ * 2 + (((lane & 7) ^ (row & 7)) * 16));
        }
#pragma unroll
        for (int i = 0; i < 3; ++i) {
            const int o = p * 2560 + i * 1024 + lane * 16;
            const int row = o >> 7, cs = (o >> 4) & 7;
            bv[i] = (unsigned)(row * S_ * 2 + ((cs ^ (row & 7)) * 16));
        }
        const int rot = G2_ROT;
        const unsigned la = smem_base + (unsigned)(p * 4096);
        const unsigned lb = smem_base + 16384u + (unsigned)(p * 2560);
#define G2_ISSUE(K, SO)                                                                 \
        do {                                                                            \
            const int kk_ = ((K) + rot) & 31;                                           \
            const u16* cb_ = Cb + kk_ * 64;                                             \
            const u16* bb_ = Bb + kk_ * 64;                                             \
            _Pragma("unroll") for (int i_ = 0; i_ < 4; ++i_) dma16(cb_, av[i_], la + (SO) + i_ * 1024u); \
            dma16(bb_, bv[0], lb + (SO));                                               \
            dma16(bb_, bv[1], lb + (SO) + 1024u);                                       \
            if (lane < 32) dma16(bb_, bv[2], lb + (SO) + 2048u);                        \
        } while (0)
#pragma unroll
        for (int k = 0; k < G2_NST; ++k) G2_ISSUE(k, (unsigned)(k * G2_SLOT));
        VM_BAR(35);
        unsigned so = 0;
        for (int k = 0; k < 32; ++k) {
            if (k <= 26) VM_BAR(28); else VM_BAR(0);
            if (k + G2_NST < 32) G2_ISSUE(k + G2_NST, so);
            so = (so == (unsigned)((G2_NST - 1) * G2_SLOT)) ? 0u : so + (unsigned)G2_SLOT;
        }
        return;
    }
    const u16 *pfp0, *pfp1;
    {
        const int l0 = w * 9 + (lane >> 3), l1 = w * 9 + 8;
        const u16* Cp = (part ? Cim : Cre) + (size_t)(t0 + ct * 16) * S_ + (lane & 7) * 64;
        const u16* Bp = abT + (size_t)(part * D_ + e0 + (loc >> 3) * 20) * S_ + (lane & 7) * 64;
        pfp0 = (l0 < 16) ? (Cp + (size_t)l0 * S_) : (Bp + (size_t)(l0 - 16) * S_);
        pfp1 = (l1 < 16) ? (Cp + (size_t)l1 * S_) : (Bp + (size_t)(l1 - 16) * S_);
    }
    unsigned pfd = 0;
#define G2_TOUCH(J)                                                                                     \
    do {                                                                                                \
        asm volatile("global_load_dword %0, %1, off" : "+v"(pfd) : "v"(pfp0 + (J) * 512) : "memory");   \
        asm volatile("global_load_dword %0, %1, off" : "+v"(pfd) : "v"(pfp1 + (J) * 512) : "memory");   \
    } while (0)
    G2_TOUCH(0); G2_TOUCH(1);
    f32x4 acc[5][2];
#pragma unroll
    for (int n = 0; n < 5; ++n)
#pragma unroll
        for (int m = 0; m < 2; ++m) acc[n][m] = f32x4{0.f, 0.f, 0.f, 0.f};
    const int rd0 = r * 128 + ((q ^ (r & 7)) * 16);
    const int rd1 = r * 128 + (((q + 4) ^ (r & 7)) * 16);
    const int aw = w * 4096;
    s16x8 cf[2][4], ef[2][10];
#define G2_READ(B, SO)                                                                  \
    do {                                                                                \
        const char* s0_ = smem + (SO) + rd0;                                            \
        const char* s1_ = smem + (SO) + rd1;                                            \
        _Pragma("unroll") for (int m_ = 0; m_ < 2; ++m_) {                              \
            cf[B][m_ * 2 + 0] = *reinterpret_cast<const s16x8*>(s0_ + aw + m_ * 2048);  \
            cf[B][m_ * 2 + 1] = *reinterpret_cast<const s16x8*>(s1_ + aw + m_ * 2048);  \
        }                                                                               \
        _Pragma("unroll") for (int n_ = 0; n_ < 5; ++n_) {                              \
            ef[B][n_ * 2 + 0] = *reinterpret_cast<const s16x8*>(s0_ + 16384 + n_ * 2048); \
            ef[B][n_ * 2 + 1] = *reinterpret_cast<const s16x8*>(s1_ + 16384 + n_ * 2048); \
        }                                                                               \
    } while (0)
#define G2_MMA(B)                                                                       \
    do {                                                                                \
        _Pragma("unroll") for (int h_ = 0; h_ < 2; ++h_)                                \
        _Pragma("unroll") for (int n_ = 0; n_ < 5; ++n_)                                \
        _Pragma("unroll") for (int m_ = 0; m_ < 2; ++m_)                                \
            acc[n_][m_] = __builtin_amdgcn_mfma_f32_16x16x32_bf16(ef[B][n_ * 2 + h_], cf[B][m_ * 2 + h_], acc[n_][m_], 0, 0, 0); \
    } while (0)
    LGKM_BAR();
    G2_READ(0, 0);
    int so = G2_SLOT;
    for (int k = 0; k < 32; k += 2) {
        FRAG_BAR(cf[0], ef[0]);
        if (k == G2_T2) G2_TOUCH(2);
        if (k == G2_T3) G2_TOUCH(3);
        G2_READ(1, so);
        so = (so == (G2_NST - 1) * G2_SLOT) ? 0 : so + G2_SLOT;
        G2_MMA(0);
        INTERLEAVE_14_20();
        FRAG_BAR(cf[1], ef[1]);
        G2_READ(0, so);
        so = (so == (G2_NST - 1) * G2_SLOT) ? 0 : so + G2_SLOT;
        G2_MMA(1);
        INTERLEAVE_14_20();
    }
    l2_touch_done1(pfd);
    u16* Pp = P + (size_t)part * S_ * D_;
#pragma unroll
    for (int m = 0; m < 2; ++m)
#pragma unroll
        for (int n = 0; n < 5; ++n) {
            const int t = t0 + w * 32 + m * 16 + r;
            const int e = e0 + n * 16 + q * 4;
            u32x2 o;
            o[0] = pack2(acc[n][m][0], acc[n][m][1]);
            o[1] = pack2(acc[n][m][2], acc[n][m][3]);
            *reinterpret_cast<u32x2*>(Pp + (size_t)t * D_ + e) = o;
        }
}

__device__ __forceinline__ unsigned topk_key(float v, int idx) {
    unsigned b = __float_as_uint(v) >> 16;
    b = (b & 0x8000u) ? (~b & 0xffffu) : (b | 0x8000u);
    return (b << 11) | (unsigned)(S_ - 1 - idx);
}
__device__ __forceinline__ unsigned umax_(unsigned a, unsigned b) { return a > b ? a : b; }
__device__ __forceinline__ unsigned wave_max_u32(unsigned v) {
    v = umax_(v, dpp_u<0x128>(v)); v = umax_(v, dpp_u<0x124>(v)); v = umax_(v, dpp_u<0x122>(v)); v = umax_(v, dpp_u<0x121>(v));
    const unsigned a = __builtin_amdgcn_readlane(v, 0), b = __builtin_amdgcn_readlane(v, 16);
    const unsigned c = __builtin_amdgcn_readlane(v, 32), d = __builtin_amdgcn_readlane(v, 48);
    return umax_(umax_(a, b), umax_(c, d));
}
typedef __attribute__((ext_vector_type(4))) short s16x4;
typedef __attribute__((address_space(3))) s16x4* lds_s16x4_p;
__device__ __forceinline__ s16x8 tr_frag(const char* img, int lane) {
    const int i = lane & 15, g = lane >> 4;
    const char* a = img + (g * 8 + (i >> 2)) * 32 + (i & 3) * 8;
    const s16x4 lo = __builtin_amdgcn_ds_read_tr16_b64_v4i16((lds_s16x4_p)(a));
    const s16x4 hi = __builtin_amdgcn_ds_read_tr16_b64_v4i16((lds_s16x4_p)(a + 128));
    return s16x8{lo[0], lo[1], lo[2], lo[3], hi[0], hi[1], hi[2], hi[3]};
}

typedef __attribute__((address_space(1))) unsigned* gu32p;
constexpr int CNT_STRIDE = 64;
__device__ __forceinline__ void st_sc1_b128(void* p, u32x4 v) {
    asm volatile("global_store_dwordx4 %0, %1, off sc1\n\ts_nop 1" :: "v"(p), "v"(v) : "memory");
}
__device__ __forceinline__ void st_sc1_b64(void* p, u32x2 v) {
    asm volatile("global_store_dwordx2 %0, %1, off sc1\n\ts_nop 1" :: "v"(p), "v"(v) : "memory");
}
__device__ __forceinline__ void st_sc1_b32(void* p, unsigned v) {
    asm volatile("global_store_dword %0, %1, off sc1\n\ts_nop 1" :: "v"(p), "v"(v) : "memory");
}
__device__ __forceinline__ void signal_wg(unsigned* cnt, int shard) {
    asm volatile("s_waitcnt vmcnt(0)" ::: "memory");
    __syncthreads();
    if (threadIdx.x == 0) __hip_atomic_fetch_add(cnt + shard * CNT_STRIDE, 1u, __ATOMIC_RELAXED, __HIP_MEMORY_SCOPE_AGENT);
}
__device__ __forceinline__ void wait_wg(unsigned* cnt, unsigned target) {
    if (threadIdx.x < 64) {
        const int lane = threadIdx.x;
        for (unsigned spins = 0; spins < (1u << 22); ++spins) {
            unsigned v = 0;
            if (lane < 8) v = __hip_atomic_load(cnt + lane * CNT_STRIDE, __ATOMIC_RELAXED, __HIP_MEMORY_SCOPE_AGENT);
            v += dpp_u<0x124>(v) ; v += dpp_u<0x122>(v); v += dpp_u<0x121>(v);
            v += dpp_u<0x128>(v);
            if (__builtin_amdgcn_readfirstlane(v) >= target) break;
            __builtin_amdgcn_s_sleep(1);
        }
    }
    __syncthreads();
}

__global__ __launch_bounds__(256) void k_tail(const u16* __restrict__ x, const u16* __restrict__ P,
                                              const u16* __restrict__ wr, const u16* __restrict__ wg,
                                              const u16* __restrict__ wu, const u16* __restrict__ wd,
                                              u16* out, float* rbuf, float2* stats, u16* act,
                                              int* sel, u16* hsel, unsigned* cnt) {
    __shared__ __attribute__((aligned(16))) char wimg[48 * 1024];
    __shared__ float red[4][2][8][16];
    const int bid = blockIdx.x;
    const int tid = threadIdx.x, lane = tid & 63, r = lane & 15, q = lane >> 4;
    const int w = __builtin_amdgcn_readfirstlane(tid >> 6);
    const bool up_role = bid < 96, dn_role = (bid >= 96) && (bid < 136);
    const int jd = bid - 96;
    const int c0 = up_role ? ((bid & 7) * 12 + (bid >> 3)) * 16 : ((jd & 7) * 5 + (jd >> 3)) * 16;
    const unsigned l0 = (unsigned)(size_t)(&wimg[0]);

    if (w >= 2) {
        if (up_role) {
            const u16* src = (w == 2) ? wg : wu;
            const unsigned voff = (unsigned)(((lane >> 1) * F_ + c0 + (lane & 1) * 8) * 2);
#pragma unroll
            for (int i = 0; i < 20; ++i) dma16(src, voff + (unsigned)(i * 32 * F_ * 2), l0 + (unsigned)((w - 2) * 20480 + i * 1024));
        } else if (dn_role) {
            const unsigned voff = (unsigned)((((w - 2) * 768 + (lane >> 1)) * D_ + c0 + (lane & 1) * 8) * 2);
#pragma unroll
            for (int i = 0; i < 24; ++i) dma16(wd, voff + (unsigned)(i * 32 * D_ * 2), l0 + (unsigned)((w - 2) * 24576 + i * 1024));
        }
    } else {
        const int row = bid * 8 + w * 4 + q;
        const size_t ro = (size_t)row * D_;
        u32x4 xv[5], p1[5], p2[5], wv[5];
#pragma unroll
        for (int c = 0; c < 5; ++c) {
            const int o = (c * 16 + r) * 8;
            xv[c] = *reinterpret_cast<const u32x4*>(x + ro + o);
            p1[c] = *reinterpret_cast<const u32x4*>(P + ro + o);
            p2[c] = *reinterpret_cast<const u32x4*>(P + (size_t)S_ * D_ + ro + o);
            wv[c] = *reinterpret_cast<const u32x4*>(wr + o);
        }
        float h[40];
        float sum = 0.f;
#pragma unroll
        for (int c = 0; c < 5; ++c)
#pragma unroll
            for (int j = 0; j < 4; ++j) {
                float a = rbf(lo_f(xv[c][j]) + rbf(lo_f(p1[c][j]) - lo_f(p2[c][j])));
                float b = rbf(hi_f(xv[c][j]) + rbf(hi_f(p1[c][j]) - hi_f(p2[c][j])));
                h[c * 8 + j * 2] = a; h[c * 8 + j * 2 + 1] = b;
                sum += a + b;
            }
        sum = red16_sum(sum);
        const float mean = sum * (1.0f / D_);
        float sq = 0.f;
#pragma unroll
        for (int i = 0; i < 40; ++i) { float d = h[i] - mean; sq += d * d; }
        sq = red16_sum(sq);
        float m_bf, rinv;
        ln_finish(sq, mean, m_bf, rinv);
        float dot = 0.f;
#pragma unroll
        for (int c = 0; c < 5; ++c) {
            u32x4 o;
#pragma unroll
            for (int j = 0; j < 4; ++j) {
                float ha = h[c * 8 + j * 2], hb = h[c * 8 + j * 2 + 1];
                float a = rbf(rbf(ha - m_bf) * rinv);
                float b = rbf(rbf(hb - m_bf) * rinv);
                dot += a * lo_f(wv[c][j]) + b * hi_f(wv[c][j]);
                o[j] = pack2(ha + a, hb + b);
            }
            st_sc1_b128(out + ro + (c * 16 + r) * 8, o);
        }
        dot = red16_sum(dot);
        if (r == 0) {
            st_sc1_b32(rbuf + row, __float_as_uint(rbf(dot)));
            u32x2 sv; sv[0] = __float_as_uint(m_bf); sv[1] = __float_as_uint(rinv);
            st_sc1_b64(stats + row, sv);
        }
    }
    signal_wg(cnt, bid & 7);
    if (!up_role && !dn_role) return;

    if (up_role) {
        wait_wg(cnt, 256u);
        f32x4 rv[8];
#pragma unroll
        for (int i = 0; i < 8; ++i)
            asm volatile("global_load_dwordx4 %0, %1, %2 sc1" : "=v"(rv[i]) : "v"((unsigned)((i * 64 + lane) * 16)), "s"(rbuf) : "memory");
        asm volatile("s_waitcnt vmcnt(0)" : "+v"(rv[0]), "+v"(rv[1]), "+v"(rv[2]), "+v"(rv[3]), "+v"(rv[4]), "+v"(rv[5]), "+v"(rv[6]), "+v"(rv[7]) :: "memory");
        unsigned key[32];
#pragma unroll
        for (int i = 0; i < 8; ++i)
#pragma unroll
            for (int j = 0; j < 4; ++j) key[i * 4 + j] = topk_key(rv[i][j], (i * 64 + lane) * 4 + j);
        unsigned prev = 1u << 27, my_key = 0;
#pragma unroll
        for (int t = 0; t < 8; ++t) {
            unsigned m = 0;
#pragma unroll
            for (int i = 0; i < 32; ++i) m = umax_(m, key[i] - prev);
            prev += wave_max_u32(m);
            my_key = (r == t) ? prev : my_key;
        }
        const int my_row = (int)(S_ - 1 - (my_key & 2047u)) & 2047;
        s16x8 af[5];
        {
            u32x2 stv; unsigned rselu;
            asm volatile("global_load_dwordx2 %0, %1, off sc1" : "=v"(stv) : "v"(stats + my_row) : "memory");
            asm volatile("global_load_dword %0, %1, off sc1" : "=v"(rselu) : "v"(rbuf + my_row) : "memory");
            u32x4 xv[5], p1[5], p2[5];
#pragma unroll
            for (int i = 0; i < 5; ++i) {
                const size_t o = (size_t)my_row * D_ + (w * 5 + i) * 32 + q * 8;
                xv[i] = *reinterpret_cast<const u32x4*>(x + o);
                p1[i] = *reinterpret_cast<const u32x4*>(P + o);
                p2[i] = *reinterpret_cast<const u32x4*>(P + (size_t)S_ * D_ + o);
            }
            asm volatile("s_waitcnt vmcnt(0)" : "+v"(stv), "+v"(rselu) :: "memory");
            const float sm = __uint_as_float(stv[0]), sr = __uint_as_float(stv[1]);
#pragma unroll
            for (int i = 0; i < 5; ++i) {
                u32x4 hp, lp;
#pragma unroll
                for (int j = 0; j < 4; ++j) {
                    const float ha = rbf(lo_f(xv[i][j]) + rbf(lo_f(p1[i][j]) - lo_f(p2[i][j])));
                    const float hb = rbf(hi_f(xv[i][j]) + rbf(hi_f(p1[i][j]) - hi_f(p2[i][j])));
                    hp[j] = pack2(ha, hb);
                    lp[j] = pack2(rbf(ha - sm) * sr, rbf(hb - sm) * sr);
                }
                if (bid == 0 && r < 8) {
                    st_sc1_b128(hsel + r * D_ + (w * 5 + i) * 32 + q * 8, hp);
                    st_sc1_b128(hsel + (8 + r) * D_ + (w * 5 + i) * 32 + q * 8, lp);
                }
                if (r >= 8) lp = u32x4{0u, 0u, 0u, 0u};
                af[i] = __builtin_bit_cast(s16x8, lp);
            }
            if (bid == 0 && w == 0 && q == 0 && r < 8) {
                u32x2 sv; sv[0] = (unsigned)my_row;
                sv[1] = __float_as_uint(rbf(1.0f / (1.0f + __expf(-__uint_as_float(rselu)))));
                st_sc1_b64(sel + 2 * r, sv);
            }
        }
        f32x4 ag = {0.f, 0.f, 0.f, 0.f}, au = {0.f, 0.f, 0.f, 0.f};
#pragma unroll
        for (int i = 0; i < 5; ++i) {
            ag = __builtin_amdgcn_mfma_f32_16x16x32_bf16(af[i], tr_frag(&wimg[(w * 5 + i) * 1024], lane), ag, 0, 0, 0);
            au = __builtin_amdgcn_mfma_f32_16x16x32_bf16(af[i], tr_frag(&wimg[20480 + (w * 5 + i) * 1024], lane), au, 0, 0, 0);
        }
        if (q < 2) {
#pragma unroll
            for (int j = 0; j < 4; ++j) { red[w][0][q * 4 + j][r] = ag[j]; red[w][1][q * 4 + j][r] = au[j]; }
        }
        __syncthreads();
        if (tid < 64) {
            const int t = tid >> 3, c = (tid & 7) * 2;
            float o2[2];
#pragma unroll
            for (int e = 0; e < 2; ++e) {
                float g = red[0][0][t][c + e] + red[1][0][t][c + e] + red[2][0][t][c + e] + red[3][0][t][c + e];
                float u = red[0][1][t][c + e] + red[1][1][t][c + e] + red[2][1][t][c + e] + red[3][1][t][c + e];
                g = rbf(g); u = rbf(u);
                o2[e] = rbf(g / (1.0f + __expf(-g))) * u;
            }
            st_sc1_b32(act + t * F_ + c0 + c, pack2(o2[0], o2[1]));
        }
        signal_wg(cnt + 8 * CNT_STRIDE, bid & 7);
        return;
    }
    wait_wg(cnt + 8 * CNT_STRIDE, 96u);
    {
        s16x8 af[12];
#pragma unroll
        for (int i = 0; i < 12; ++i)
            asm volatile("global_load_dwordx4 %0, %1, off sc1" : "=v"(af[i]) : "v"(act + (r & 7) * F_ + (w * 12 + i) * 32 + q * 8) : "memory");
        const int et = (tid >> 3) & 7, ec = (tid & 7) * 2;
        unsigned hv2, ln2v; u32x2 sv;
        asm volatile("global_load_dword %0, %1, off sc1" : "=v"(hv2) : "v"(hsel + et * D_ + c0 + ec) : "memory");
        asm volatile("global_load_dword %0, %1, off sc1" : "=v"(ln2v) : "v"(hsel + (8 + et) * D_ + c0 + ec) : "memory");
        asm volatile("global_load_dwordx2 %0, %1, off sc1" : "=v"(sv) : "v"(sel + 2 * et) : "memory");
        asm volatile("s_waitcnt vmcnt(0)"
                     : "+v"(af[0]), "+v"(af[1]), "+v"(af[2]), "+v"(af[3]), "+v"(af[4]), "+v"(af[5]), "+v"(af[6]), "+v"(af[7]),
                       "+v"(af[8]), "+v"(af[9]), "+v"(af[10]), "+v"(af[11]), "+v"(hv2), "+v"(ln2v), "+v"(sv) :: "memory");
        f32x4 a = {0.f, 0.f, 0.f, 0.f};
#pragma unroll
        for (int i = 0; i < 12; ++i) a = __builtin_amdgcn_mfma_f32_16x16x32_bf16(af[i], tr_frag(&wimg[(w * 12 + i) * 1024], lane), a, 0, 0, 0);
        if (q < 2) {
#pragma unroll
            for (int j = 0; j < 4; ++j) red[w][0][q * 4 + j][r] = a[j];
        }
        __syncthreads();
        if (tid < 64) {
            const float sg = __uint_as_float(sv[1]);
            float o2[2];
#pragma unroll
            for (int e = 0; e < 2; ++e) {
                float y = red[0][0][et][ec + e] + red[1][0][et][ec + e] + red[2][0][et][ec + e] + red[3][0][et][ec + e];
                y = rbf(rbf(y) * sg);
                const float hv = e ? hi_f(hv2) : lo_f(hv2), ln = e ? hi_f(ln2v) : lo_f(ln2v);
                o2[e] = hv + rbf(ln + y);
            }
            st_sc1_b32(out + (size_t)(sv[0] & 2047u) * D_ + c0 + ec, pack2(o2[0], o2[1]));
        }
    }
}

extern "C" void kernel_launch(void* const* d_in, const int* in_sizes, int n_in,
                              void* d_out, int out_size, void* d_ws, size_t ws_size,
                              hipStream_t stream) {
    const u16* x   = (const u16*)d_in[0];
    const u16* Cre = (const u16*)d_in[1];
    const u16* Cim = (const u16*)d_in[2];
    const u16* Wre = (const u16*)d_in[3];
    const u16* Wim = (const u16*)d_in[4];
    const u16* wr  = (const u16*)d_in[6];
    const u16* wg  = (const u16*)d_in[7];
    const u16* wu  = (const u16*)d_in[8];
    const u16* wd  = (const u16*)d_in[9];
    u16* out = (u16*)d_out;

    char* ws = (char*)d_ws;
    u16* abT = (u16*)ws;
    u16* P = (u16*)(ws + (size_t)2 * D_ * S_ * 2);
    char* tail = ws + (size_t)4 * D_ * S_ * 2;
    float* rbuf = (float*)tail;
    float2* stats = (float2*)(tail + 8192);
    u16* act = (u16*)(tail + 8192 + 16384);
    int* sel = (int*)(tail + 8192 + 16384 + 24576);
    u16* hsel = (u16*)(tail + 8192 + 16384 + 24576 + 128);
    unsigned* cnt = (unsigned*)(tail + 8192 + 16384 + 24576 + 128 + 20480 + 128);

    (void)hipFuncSetAttribute((const void*)k_gemm1, hipFuncAttributeMaxDynamicSharedMemorySize, G1_LDS);
    (void)hipFuncSetAttribute((const void*)k_gemm2, hipFuncAttributeMaxDynamicSharedMemorySize, G2_LDS);

    k_gemm1<<<256, 512, G1_LDS, stream>>>(x, Wre, Wim, abT, cnt);
    k_gemm2<<<256, 512, G2_LDS, stream>>>(Cre, Cim, abT, P);
    k_tail<<<256, 256, 0, stream>>>(x, P, wr, wg, wu, wd, out, rbuf, stats, act, sel, hsel, cnt);
}
```

```cpp
#include <hip/hip_runtime.h>
#include <hip/hip_bf16.h>

typedef unsigned short u16;
typedef __attribute__((ext_vector_type(8))) short s16x8;
typedef __attribute__((ext_vector_type(4))) float f32x4;
typedef __attribute__((ext_vector_type(4))) unsigned int u32x4;
typedef __attribute__((ext_vector_type(2))) unsigned int u32x2;

constexpr int S_ = 2048, D_ = 640, F_ = 1536, C_ = 8;

__device__ __forceinline__ float bf2f(u16 v) { return __uint_as_float(((unsigned)v) << 16); }
__device__ __forceinline__ u16 f2bf(float f) { __bf16 b = (__bf16)f; return __builtin_bit_cast(u16, b); }
__device__ __forceinline__ float rbf(float f) { return bf2f(f2bf(f)); }
__device__ __forceinline__ float lo_f(unsigned w) { return __uint_as_float(w << 16); }
__device__ __forceinline__ float hi_f(unsigned w) { return __uint_as_float(w & 0xffff0000u); }
__device__ __forceinline__ unsigned pack2(float a, float b) { return (unsigned)f2bf(a) | ((unsigned)f2bf(b) << 16); }

template <int CTRL> __device__ __forceinline__ unsigned dpp_u(unsigned v) {
    return (unsigned)__builtin_amdgcn_update_dpp(0, (int)v, CTRL, 0xf, 0xf, false);
}
template <int CTRL> __device__ __forceinline__ float dpp_f(float v) { return __uint_as_float(dpp_u<CTRL>(__float_as_uint(v))); }
__device__ __forceinline__ float red16_sum(float v) {
    v += dpp_f<0x128>(v); v += dpp_f<0x124>(v); v += dpp_f<0x122>(v); v += dpp_f<0x121>(v);
    return v;
}

__device__ __forceinline__ void ln_finish(float sum_sq_dev, float mean, float& m_bf, float& rinv) {
    float v = rbf(sum_sq_dev * (1.0f / D_));
    float den = rbf(sqrtf(rbf(v + 1e-5f)));
    m_bf = rbf(mean);
    rinv = 1.0f / den;
}

__device__ __forceinline__ void dma16(const u16* base, unsigned voff, unsigned ldsaddr) {
    unsigned keep;
    asm volatile("s_mov_b32 %0, m0\n\ts_mov_b32 m0, %3\n\ts_nop 0\n\tglobal_load_lds_dwordx4 %1, %2\n\ts_mov_b32 m0, %0"
                 : "=&s"(keep) : "v"(voff), "s"(base), "s"(ldsaddr) : "memory");
}
__device__ __forceinline__ unsigned l2_touch(const u16* base, unsigned voff) {
    unsigned d;
    asm volatile("global_load_dword %0, %1, %2" : "=v"(d) : "v"(voff), "s"(base) : "memory");
    return d;
}
#define l2_touch_done5(a, b, c, d, e) asm volatile("s_waitcnt vmcnt(0)" : "+v"(a), "+v"(b), "+v"(c), "+v"(d), "+v"(e) :: "memory")
#define l2_touch_done1(a) asm volatile("s_waitcnt vmcnt(0)" : "+v"(a) :: "memory")
#define LGKM_BAR() asm volatile("s_waitcnt lgkmcnt(0)\n\ts_barrier" ::: "memory")
#define VM_BAR(N) asm volatile("s_waitcnt vmcnt(" #N ")\n\ts_barrier" ::: "memory")
#define FRAG_BAR(A, B)                                                                              \
    asm volatile("s_waitcnt lgkmcnt(0)\n\ts_barrier"                                                \
                 : "+v"(A[0]), "+v"(A[1]), "+v"(A[2]), "+v"(A[3]), "+v"(B[0]), "+v"(B[1]), "+v"(B[2]), \
                   "+v"(B[3]), "+v"(B[4]), "+v"(B[5]), "+v"(B[6]), "+v"(B[7]), "+v"(B[8]), "+v"(B[9]) \
                 :: "memory")
#define INTERLEAVE_14_20()                                                              \
    do {                                                                                \
        _Pragma("unroll") for (int i_ = 0; i_ < 14; ++i_) {                             \
            __builtin_amdgcn_sched_group_barrier(0x100, 1, 0);                          \
            __builtin_amdgcn_sched_group_barrier(0x008, 1, 0);                          \
        }                                                                               \
        __builtin_amdgcn_sched_group_barrier(0x008, 6, 0);                              \
        __builtin_amdgcn_sched_barrier(0);                                              \
    } while (0)

constexpr int G1_XN = 64 * 1280;
constexpr int G1_SLOT = 160 * 128;
constexpr int G1_LDS = G1_XN + 4 * G1_SLOT;

__global__ __launch_bounds__(512, 2) void k_gemm1(const u16* __restrict__ x, const u16* __restrict__ Wre,
                                                  const u16* __restrict__ Wim, u16* __restrict__ abT,
                                                  u32x4* __restrict__ clr) {
    extern __shared__ __attribute__((aligned(16))) char smem[];
    const int bid = blockIdx.x, xcd = bid & 7, loc = bid >> 3;
    if (bid < 9) {
        const int i = bid * 512 + threadIdx.x;
        if (i < (16384 + 49152 + 2048) / 16) clr[i] = u32x4{0u, 0u, 0u, 0u};
    }
    const int st = (xcd >> 1) * 8 + (loc & 7);
    const int et = (xcd & 1) * 4 + (loc >> 3);
    const int s0 = st * 64, e0 = et * 160;
    const u16* W = (et < 4) ? (Wre + (size_t)e0 * D_) : (Wim + (size_t)(e0 - D_) * D_);
    const int tid = threadIdx.x, lane = tid & 63;
    const int w = __builtin_amdgcn_readfirstlane(tid >> 6);
    const int r = lane & 15, q = lane >> 4;
    const unsigned smem_base = (unsigned)(size_t)(smem);

    u32x4 xv[2][5];
#pragma unroll
    for (int i = 0; i < 2; ++i) {
        const u16* xr = x + (size_t)(s0 + w * 8 + i * 4 + q) * D_;
#pragma unroll
        for (int c = 0; c < 5; ++c) xv[i][c] = *reinterpret_cast<const u32x4*>(xr + (c * 16 + r) * 8);
    }
    unsigned wv[5];
    const int p = w - 4;
#pragma unroll
    for (int i = 0; i < 5; ++i) {
        const int row = (p & 3) * 40 + i * 8 + (lane >> 3);
        wv[i] = (unsigned)(row * D_ * 2 + (((lane & 7) ^ (row & 7)) * 16));
    }
    const unsigned wl = smem_base + G1_XN + (unsigned)((p & 3) * 5120);
#define G1_ISSUE(K)                                                                     \
    do {                                                                                \
        _Pragma("unroll") for (int i_ = 0; i_ < 5; ++i_)                                \
            dma16(W + (K) * 64, wv[i_], wl + (unsigned)(((K) & 3) * G1_SLOT + i_ * 1024)); \
    } while (0)
    if (w >= 4) { G1_ISSUE(0); G1_ISSUE(1); G1_ISSUE(2); G1_ISSUE(3); }
    unsigned pf = 0;
    if (w < 4) {
        int li = w * 64 + lane;
        li = li < 200 ? li : 199;
        pf = l2_touch(W + (size_t)((loc & 7) * 20) * D_, (unsigned)((li % 20) * D_ * 2 + (li / 20) * 128));
    }

#pragma unroll
    for (int i = 0; i < 2; ++i) {
        float sum = 0.f;
#pragma unroll
        for (int c = 0; c < 5; ++c)
#pragma unroll
            for (int j = 0; j < 4; ++j) sum += lo_f(xv[i][c][j]) + hi_f(xv[i][c][j]);
        sum = red16_sum(sum);
        const float mean = sum * (1.0f / D_);
        float sq = 0.f;
#pragma unroll
        for (int c = 0; c < 5; ++c)
#pragma unroll
            for (int j = 0; j < 4; ++j) {
                float a = lo_f(xv[i][c][j]) - mean, b = hi_f(xv[i][c][j]) - mean;
                sq += a * a + b * b;
            }
        sq = red16_sum(sq);
        float m_bf, rinv;
        ln_finish(sq, mean, m_bf, rinv);
        const int row = w * 8 + i * 4 + q;
#pragma unroll
        for (int c = 0; c < 5; ++c) {
            u32x4 o;
#pragma unroll
            for (int j = 0; j < 4; ++j) {
                float a = rbf(lo_f(xv[i][c][j]) - m_bf) * rinv;
                float b = rbf(hi_f(xv[i][c][j]) - m_bf) * rinv;
                o[j] = pack2(a, b);
            }
            const int ch = (c * 16 + r) ^ (row & 15);
            *reinterpret_cast<u32x4*>(smem + row * 1280 + ch * 16) = o;
        }
    }

    if (w >= 4) {
        asm volatile("s_waitcnt vmcnt(15) lgkmcnt(0)\n\ts_barrier" ::: "memory");
#pragma unroll
        for (int k = 0; k < 10; ++k) {
            if (k <= 6) VM_BAR(10); else VM_BAR(0);
            if (k + 4 < 10) G1_ISSUE(k + 4);
        }
        return;
    }
    l2_touch_done1(pf);
    const int ws = w & 1, we = w >> 1;
    f32x4 acc[2][5];
#pragma unroll
    for (int m = 0; m < 2; ++m)
#pragma unroll
        for (int n = 0; n < 5; ++n) acc[m][n] = f32x4{0.f, 0.f, 0.f, 0.f};
    const char* xbase = smem + (ws * 32 + r) * 1280;
    const char* wbase = smem + G1_XN + (we * 80 + r) * 128;
    s16x8 xf[2][4], wf[2][10];
#define G1_READ(B, K)                                                                   \
    do {                                                                                \
        _Pragma("unroll") for (int h_ = 0; h_ < 2; ++h_) {                              \
            _Pragma("unroll") for (int m_ = 0; m_ < 2; ++m_)                            \
                xf[B][m_ * 2 + h_] = *reinterpret_cast<const s16x8*>(xbase + m_ * 16 * 1280 + ((((K) * 8 + h_ * 4 + q) ^ r) * 16)); \
            _Pragma("unroll") for (int n_ = 0; n_ < 5; ++n_)                            \
                wf[B][n_ * 2 + h_] = *reinterpret_cast<const s16x8*>(wbase + ((K) & 3) * G1_SLOT + n_ * 2048 + (((h_ * 4 + q) ^ (r & 7)) * 16)); \
        }                                                                               \
    } while (0)
    LGKM_BAR();
    G1_READ(0, 0);
#pragma unroll
    for (int k = 0; k < 10; ++k) {
        FRAG_BAR(xf[k & 1], wf[k & 1]);
        if (k + 1 < 10) G1_READ((k + 1) & 1, k + 1);
#pragma unroll
        for (int h = 0; h < 2; ++h)
#pragma unroll
            for (int m = 0; m < 2; ++m)
#pragma unroll
                for (int n = 0; n < 5; ++n)
                    acc[m][n] = __builtin_amdgcn_mfma_f32_16x16x32_bf16(xf[k & 1][m * 2 + h], wf[k & 1][n * 2 + h], acc[m][n], 0, 0, 0);
        if (k + 1 < 10) INTERLEAVE_14_20();
    }
#pragma unroll
    for (int m = 0; m < 2; ++m)
#pragma unroll
        for (int n = 0; n < 5; ++n) {
            const int e = e0 + we * 80 + n * 16 + r;
            const int s = s0 + ws * 32 + m * 16 + q * 4;
            u32x2 o;
            o[0] = pack2(acc[m][n][0], acc[m][n][1]);
            o[1] = pack2(acc[m][n][2], acc[m][n][3]);
            *reinterpret_cast<u32x2*>(abT + (size_t)e * S_ + s) = o;
        }
}

#define G2_ROT 0
#define G2_T2 8
#define G2_T3 16
constexpr int G2_SLOT = (128 + 80) * 128;
constexpr int G2_NST = 6;
constexpr int G2_LDS = G2_NST * G2_SLOT;

__global__ __launch_bounds__(512, 2) void k_gemm2(const u16* __restrict__ Cre, const u16* __restrict__ Cim,
                                                  const u16* __restrict__ abT, u16* __restrict__ P) {
    extern __shared__ __attribute__((aligned(16))) char smem[];
    const int bid = blockIdx.x, xcd = bid & 7, loc = bid >> 3;
    const int part = xcd & 1;
    const int tp = (xcd >> 1) * 4 + (loc >> 3);
    const int ct = loc & 7;
    const int t0 = tp * 128, e0 = ct * 80;
    const int tid = threadIdx.x, lane = tid & 63;
    const int w = __builtin_amdgcn_readfirstlane(tid >> 6);
    const int r = lane & 15, q = lane >> 4;
    const unsigned smem_base = (unsigned)(size_t)(smem);

    if (w >= 4) {
        const int p = w - 4;
        const u16* Cb = (part ? Cim : Cre) + (size_t)t0 * S_;
        const u16* Bb = abT + (size_t)(part * D_ + e0) * S_;
        unsigned av[4], bv[3];
#pragma unroll
        for (int i = 0; i < 4; ++i) {
            const int row = p * 32 + i * 8 + (lane >> 3);
            av[i] = (unsigned)(row * S_ * 2 + (((lane & 7) ^ (row & 7)) * 16));
        }
#pragma unroll
        for (int i = 0; i < 3; ++i) {
            const int o = p * 2560 + i * 1024 + lane * 16;
            const int row = o >> 7, cs = (o >> 4) & 7;
            bv[i] = (unsigned)(row * S_ * 2 + ((cs ^ (row & 7)) * 16));
        }
        const int rot = G2_ROT;
        const unsigned la = smem_base + (unsigned)(p * 4096);
        const unsigned lb = smem_base + 16384u + (unsigned)(p * 2560);
#define G2_ISSUE(K, SO)                                                                 \
        do {                                                                            \
            const int kk_ = ((K) + rot) & 31;                                           \
            const u16* cb_ = Cb + kk_ * 64;                                             \
            const u16* bb_ = Bb + kk_ * 64;                                             \
            _Pragma("unroll") for (int i_ = 0; i_ < 4; ++i_) dma16(cb_, av[i_], la + (SO) + i_ * 1024u); \
            dma16(bb_, bv[0], lb + (SO));                                               \
            dma16(bb_, bv[1], lb + (SO) + 1024u);                                       \
            if (lane < 32) dma16(bb_, bv[2], lb + (SO) + 2048u);                        \
        } while (0)
#pragma unroll
        for (int k = 0; k < G2_NST; ++k) G2_ISSUE(k, (unsigned)(k * G2_SLOT));
        VM_BAR(35);
        unsigned so = 0;
        for (int k = 0; k < 32; ++k) {
            if (k <= 26) VM_BAR(28); else VM_BAR(0);
            if (k + G2_NST < 32) G2_ISSUE(k + G2_NST, so);
            so = (so == (unsigned)((G2_NST - 1) * G2_SLOT)) ? 0u : so + (unsigned)G2_SLOT;
        }
        return;
    }
    const u16 *pfp0, *pfp1;
    {
        const int l0 = w * 9 + (lane >> 3), l1 = w * 9 + 8;
        const u16* Cp = (part ? Cim : Cre) + (size_t)(t0 + ct * 16) * S_ + (lane & 7) * 64;
        const u16* Bp = abT + (size_t)(part * D_ + e0 + (loc >> 3) * 20) * S_ + (lane & 7) * 64;
        pfp0 = (l0 < 16) ? (Cp + (size_t)l0 * S_) : (Bp + (size_t)(l0 - 16) * S_);
        pfp1 = (l1 < 16) ? (Cp + (size_t)l1 * S_) : (Bp + (size_t)(l1 - 16) * S_);
    }
    unsigned pfd = 0;
#define G2_TOUCH(J)                                                                                     \
    do {                                                                                                \
        asm volatile("global_load_dword %0, %1, off" : "+v"(pfd) : "v"(pfp0 + (J) * 512) : "memory");   \
        asm volatile("global_load_dword %0, %1, off" : "+v"(pfd) : "v"(pfp1 + (J) * 512) : "memory");   \
    } while (0)
    G2_TOUCH(0); G2_TOUCH(1);
    f32x4 acc[5][2];
#pragma unroll
    for (int n = 0; n < 5; ++n)
#pragma unroll
        for (int m = 0; m < 2; ++m) acc[n][m] = f32x4{0.f, 0.f, 0.f, 0.f};
    const int rd0 = r * 128 + ((q ^ (r & 7)) * 16);
    const int rd1 = r * 128 + (((q + 4) ^ (r & 7)) * 16);
    const int aw = w * 4096;
    s16x8 cf[2][4], ef[2][10];
#define G2_READ(B, SO)                                                                  \
    do {                                                                                \
        const char* s0_ = smem + (SO) + rd0;                                            \
        const char* s1_ = smem + (SO) + rd1;                                            \
        _Pragma("unroll") for (int m_ = 0; m_ < 2; ++m_) {                              \
            cf[B][m_ * 2 + 0] = *reinterpret_cast<const s16x8*>(s0_ + aw + m_ * 2048);  \
            cf[B][m_ * 2 + 1] = *reinterpret_cast<const s16x8*>(s1_ + aw + m_ * 2048);  \
        }                                                                               \
        _Pragma("unroll") for (int n_ = 0; n_ < 5; ++n_) {                              \
            ef[B][n_ * 2 + 0] = *reinterpret_cast<const s16x8*>(s0_ + 16384 + n_ * 2048); \
            ef[B][n_ * 2 + 1] = *reinterpret_cast<const s16x8*>(s1_ + 16384 + n_ * 2048); \
        }                                                                               \
    } while (0)
#define G2_MMA(B)                                                                       \
    do {                                                                                \
        _Pragma("unroll") for (int h_ = 0; h_ < 2; ++h_)                                \
        _Pragma("unroll") for (int n_ = 0; n_ < 5; ++n_)                                \
        _Pragma("unroll") for (int m_ = 0; m_ < 2; ++m_)                                \
            acc[n_][m_] = __builtin_amdgcn_mfma_f32_16x16x32_bf16(ef[B][n_ * 2 + h_], cf[B][m_ * 2 + h_], acc[n_][m_], 0, 0, 0); \
    } while (0)
    LGKM_BAR();
    G2_READ(0, 0);
    int so = G2_SLOT;
    for (int k = 0; k < 32; k += 2) {
        FRAG_BAR(cf[0], ef[0]);
        if (k == G2_T2) G2_TOUCH(2);
        if (k == G2_T3) G2_TOUCH(3);
        G2_READ(1, so);
        so = (so == (G2_NST - 1) * G2_SLOT) ? 0 : so + G2_SLOT;
        G2_MMA(0);
        INTERLEAVE_14_20();
        FRAG_BAR(cf[1], ef[1]);
        G2_READ(0, so);
        so = (so == (G2_NST - 1) * G2_SLOT) ? 0 : so + G2_SLOT;
        G2_MMA(1);
        INTERLEAVE_14_20();
    }
    l2_touch_done1(pfd);
    u16* Pp = P + (size_t)part * S_ * D_;
#pragma unroll
    for (int m = 0; m < 2; ++m)
#pragma unroll
        for (int n = 0; n < 5; ++n) {
            const int t = t0 + w * 32 + m * 16 + r;
            const int e = e0 + n * 16 + q * 4;
            u32x2 o;
            o[0] = pack2(acc[n][m][0], acc[n][m][1]);
            o[1] = pack2(acc[n][m][2], acc[n][m][3]);
            *reinterpret_cast<u32x2*>(Pp + (size_t)t * D_ + e) = o;
        }
}

__device__ __forceinline__ unsigned topk_key(float v, int idx) {
    unsigned b = __float_as_uint(v) >> 16;
    b = (b & 0x8000u) ? (~b & 0xffffu) : (b | 0x8000u);
    return (b << 11) | (unsigned)(S_ - 1 - idx);
}
__device__ __forceinline__ unsigned umax_(unsigned a, unsigned b) { return a > b ? a : b; }
__device__ __forceinline__ unsigned wave_max_u32(unsigned v) {
    v = umax_(v, dpp_u<0x128>(v)); v = umax_(v, dpp_u<0x124>(v)); v = umax_(v, dpp_u<0x122>(v)); v = umax_(v, dpp_u<0x121>(v));
    const unsigned a = __builtin_amdgcn_readlane(v, 0), b = __builtin_amdgcn_readlane(v, 16);
    const unsigned c = __builtin_amdgcn_readlane(v, 32), d = __builtin_amdgcn_readlane(v, 48);
    return umax_(umax_(a, b), umax_(c, d));
}
typedef __attribute__((ext_vector_type(4))) short s16x4;
typedef __attribute__((address_space(3))) s16x4* lds_s16x4_p;
__device__ __forceinline__ s16x8 tr_frag(const char* img, int lane) {
    const int i = lane & 15, g = lane >> 4;
    const char* a = img + (g * 8 + (i >> 2)) * 32 + (i & 3) * 8;
    const s16x4 lo = __builtin_amdgcn_ds_read_tr16_b64_v4i16((lds_s16x4_p)(a));
    const s16x4 hi = __builtin_amdgcn_ds_read_tr16_b64_v4i16((lds_s16x4_p)(a + 128));
    return s16x8{lo[0], lo[1], lo[2], lo[3], hi[0], hi[1], hi[2], hi[3]};
}

constexpr int CNT_STRIDE = 64;
constexpr unsigned RG_TAG = 0x5AA5u;
constexpr unsigned ACT_TAG = 0x7C3E91B5u;
constexpr unsigned SPIN_CAP = 1u << 20;
__device__ __forceinline__ void st_sc1_b128(void* p, u32x4 v) {
    asm volatile("global_store_dwordx4 %0, %1, off sc1\n\ts_nop 1" :: "v"(p), "v"(v) : "memory");
}
__device__ __forceinline__ void st_sc1_b64(void* p, u32x2 v) {
    asm volatile("global_store_dwordx2 %0, %1, off sc1\n\ts_nop 1" :: "v"(p), "v"(v) : "memory");
}
__device__ __forceinline__ void st_sc1_b32(void* p, unsigned v) {
    asm volatile("global_store_dword %0, %1, off sc1\n\ts_nop 1" :: "v"(p), "v"(v) : "memory");
}

__device__ __forceinline__ int route_top8(const u32x2* rg, unsigned* cand, u32x2* lrg, int w, int lane, u32x2& pay) {
    u32x4 g[4];
    for (unsigned spins = 0; spins < SPIN_CAP; ++spins) {
#pragma unroll
        for (int i = 0; i < 4; ++i)
            asm volatile("global_load_dwordx4 %0, %1, %2 sc1" : "=v"(g[i]) : "v"((unsigned)((w * 256 + i * 64 + lane) * 16)), "s"(rg) : "memory");
        asm volatile("s_waitcnt vmcnt(0)" : "+v"(g[0]), "+v"(g[1]), "+v"(g[2]), "+v"(g[3]) :: "memory");
        bool ok = true;
#pragma unroll
        for (int i = 0; i < 4; ++i) ok = ok && ((g[i][1] >> 16) == RG_TAG) && ((g[i][3] >> 16) == RG_TAG);
        if (__all(ok)) break;
        __builtin_amdgcn_s_sleep(2);
    }
    unsigned key[8];
#pragma unroll
    for (int i = 0; i < 4; ++i) {
        const int idx = (w * 256 + i * 64 + lane) * 2;
        key[2 * i] = topk_key(lo_f(g[i][0]), idx);
        key[2 * i + 1] = topk_key(lo_f(g[i][2]), idx + 1);
        *reinterpret_cast<u32x4*>(lrg + idx) = g[i];
    }
    unsigned prev = 1u << 27, my_key = 0;
#pragma unroll
    for (int t = 0; t < 8; ++t) {
        unsigned m = 0;
#pragma unroll
        for (int i = 0; i < 8; ++i) m = umax_(m, key[i] - prev);
        prev += wave_max_u32(m);
        my_key = (lane == t) ? prev : my_key;
    }
    if (lane < 8) cand[w * 8 + lane] = my_key;
    __syncthreads();
    const unsigned ck = cand[lane & 31];
    prev = 1u << 27; my_key = 0;
#pragma unroll
    for (int t = 0; t < 8; ++t) {
        prev += wave_max_u32(ck - prev);
        my_key = ((lane & 7) == t) ? prev : my_key;
    }
    const int row = (S_ - 1 - (int)(my_key & 2047u)) & 2047;
    pay = lrg[row];
    return row;
}

__global__ __launch_bounds__(256) void k_tail(const u16* __restrict__ x, const u16* __restrict__ P,
                                              const u16* __restrict__ wr, const u16* __restrict__ wg,
                                              const u16* __restrict__ wu, const u16* __restrict__ wd,
                                              u16* out, u32x2* rg, u32x2* actg, unsigned* cnt) {
    __shared__ __attribute__((aligned(16))) char wimg[48 * 1024];
    __shared__ __attribute__((aligned(16))) u32x2 lrg[2048];
    __shared__ float red[4][2][8][16];
    __shared__ unsigned cand[32];
    __shared__ unsigned arrived;
    const int bid = blockIdx.x;
    const int tid = threadIdx.x, lane = tid & 63, r = lane & 15, q = lane >> 4;
    const int w = __builtin_amdgcn_readfirstlane(tid >> 6);
    const unsigned l0 = (unsigned)(size_t)(&wimg[0]);

    if (bid >= 128) {
        const int ju = bid - 128;
        const int c0 = ((ju & 7) * 12 + (ju >> 3)) * 16;
        {
            const unsigned voff = (unsigned)(((w * 160 + (lane >> 1)) * F_ + c0 + (lane & 1) * 8) * 2);
#pragma unroll
            for (int i = 0; i < 5; ++i) {
                dma16(wg, voff + (unsigned)(i * 32 * F_ * 2), l0 + (unsigned)((w * 5 + i) * 1024));
                dma16(wu, voff + (unsigned)(i * 32 * F_ * 2), l0 + (unsigned)(20480 + (w * 5 + i) * 1024));
            }
        }
        u32x2 pay;
        const int my_row = route_top8(rg, cand, lrg, w, lane, pay);
        u32x4 xv[5], p1[5], p2[5];
#pragma unroll
        for (int i = 0; i < 5; ++i) {
            const size_t o = (size_t)my_row * D_ + (w * 5 + i) * 32 + q * 8;
            xv[i] = *reinterpret_cast<const u32x4*>(x + o);
            p1[i] = *reinterpret_cast<const u32x4*>(P + o);
            p2[i] = *reinterpret_cast<const u32x4*>(P + (size_t)S_ * D_ + o);
        }
        const float sm = hi_f(pay[0]), sr = 1.0f / lo_f(pay[1]);
        f32x4 ag = {0.f, 0.f, 0.f, 0.f}, au = {0.f, 0.f, 0.f, 0.f};
#pragma unroll
        for (int i = 0; i < 5; ++i) {
            u32x4 lp;
#pragma unroll
            for (int j = 0; j < 4; ++j) {
                const float ha = rbf(lo_f(xv[i][j]) + rbf(lo_f(p1[i][j]) - lo_f(p2[i][j])));
                const float hb = rbf(hi_f(xv[i][j]) + rbf(hi_f(p1[i][j]) - hi_f(p2[i][j])));
                lp[j] = pack2(rbf(ha - sm) * sr, rbf(hb - sm) * sr);
            }
            const s16x8 af = __builtin_bit_cast(s16x8, lp);
            ag = __builtin_amdgcn_mfma_f32_16x16x32_bf16(af, tr_frag(&wimg[(w * 5 + i) * 1024], lane), ag, 0, 0, 0);
            au = __builtin_amdgcn_mfma_f32_16x16x32_bf16(af, tr_frag(&wimg[20480 + (w * 5 + i) * 1024], lane), au, 0, 0, 0);
        }
        if (q < 2) {
#pragma unroll
            for (int j = 0; j < 4; ++j) { red[w][0][q * 4 + j][r] = ag[j]; red[w][1][q * 4 + j][r] = au[j]; }
        }
        __syncthreads();
        if (tid < 64) {
            const int t = tid >> 3, c = (tid & 7) * 2;
            float o2[2];
#pragma unroll
            for (int e = 0; e < 2; ++e) {
                float g = red[0][0][t][c + e] + red[1][0][t][c + e] + red[2][0][t][c + e] + red[3][0][t][c + e];
                float u = red[0][1][t][c + e] + red[1][1][t][c + e] + red[2][1][t][c + e] + red[3][1][t][c + e];
                g = rbf(g); u = rbf(u);
                o2[e] = rbf(g / (1.0f + __expf(-g))) * u;
            }
            u32x2 gv; gv[0] = pack2(o2[0], o2[1]); gv[1] = ACT_TAG;
            st_sc1_b64(actg + t * (F_ / 2) + (c0 + c) / 2, gv);
        }
        return;
    }

    if (tid == 0) arrived = 0u;
    __syncthreads();
    {
        const int row = bid * 16 + w * 4 + q;
        const size_t ro = (size_t)row * D_;
        u32x4 xv[5], p1[5], p2[5], wv[5];
#pragma unroll
        for (int c = 0; c < 5; ++c) {
            const int o = (c * 16 + r) * 8;
            xv[c] = *reinterpret_cast<const u32x4*>(x + ro + o);
            p1[c] = *reinterpret_cast<const u32x4*>(P + ro + o);
            p2[c] = *reinterpret_cast<const u32x4*>(P + (size_t)S_ * D_ + ro + o);
            wv[c] = *reinterpret_cast<const u32x4*>(wr + o);
        }
        float h[40];
        float sum = 0.f;
#pragma unroll
        for (int c = 0; c < 5; ++c)
#pragma unroll
            for (int j = 0; j < 4; ++j) {
                float a = rbf(lo_f(xv[c][j]) + rbf(lo_f(p1[c][j]) - lo_f(p2[c][j])));
                float b = rbf(hi_f(xv[c][j]) + rbf(hi_f(p1[c][j]) - hi_f(p2[c][j])));
                h[c * 8 + j * 2] = a; h[c * 8 + j * 2 + 1] = b;
                sum += a + b;
            }
        sum = red16_sum(sum);
        const float mean = sum * (1.0f / D_);
        float sq = 0.f;
#pragma unroll
        for (int i = 0; i < 40; ++i) { float d = h[i] - mean; sq += d * d; }
        sq = red16_sum(sq);
        const float m_bf = rbf(mean);
        const float den = rbf(sqrtf(rbf(rbf(sq * (1.0f / D_)) + 1e-5f)));
        const float rinv = 1.0f / den;
        float dot = 0.f;
        u32x4 o[5];
#pragma unroll
        for (int c = 0; c < 5; ++c)
#pragma unroll
            for (int j = 0; j < 4; ++j) {
                float ha = h[c * 8 + j * 2], hb = h[c * 8 + j * 2 + 1];
                float a = rbf(rbf(ha - m_bf) * rinv);
                float b = rbf(rbf(hb - m_bf) * rinv);
                dot += a * lo_f(wv[c][j]) + b * hi_f(wv[c][j]);
                o[c][j] = pack2(ha + a, hb + b);
            }
        dot = red16_sum(dot);
        if (r == 0) {
            u32x2 gv; gv[0] = pack2(dot, m_bf); gv[1] = (unsigned)f2bf(den) | (RG_TAG << 16);
            st_sc1_b64(rg + row, gv);
        }
#pragma unroll
        for (int c = 0; c < 5; ++c) st_sc1_b128(out + ro + (c * 16 + r) * 8, o[c]);
        asm volatile("s_waitcnt vmcnt(0)" ::: "memory");
        if (lane == 0) {
            const unsigned old = __hip_atomic_fetch_add(&arrived, 1u, __ATOMIC_RELAXED, __HIP_MEMORY_SCOPE_WORKGROUP);
            if (old == 3u) __hip_atomic_fetch_add(cnt + (bid & 7) * CNT_STRIDE, 1u, __ATOMIC_RELAXED, __HIP_MEMORY_SCOPE_AGENT);
        }
    }
    if (bid >= 40) return;

    const int c0 = ((bid & 7) * 5 + (bid >> 3)) * 16;
    {
        const unsigned voff = (unsigned)(((w * 384 + (lane >> 1)) * D_ + c0 + (lane & 1) * 8) * 2);
#pragma unroll
        for (int i = 0; i < 12; ++i) dma16(wd, voff + (unsigned)(i * 32 * D_ * 2), l0 + (unsigned)((w * 12 + i) * 1024));
    }
    u32x2 pay;
    const int my_row = route_top8(rg, cand, lrg, w, lane, pay);
    float hv[2], lv[2], sg;
    {
        const size_t o = (size_t)my_row * D_ + c0 + (lane >> 3) * 2;
        const unsigned xv = *reinterpret_cast<const unsigned*>(x + o);
        const unsigned p1 = *reinterpret_cast<const unsigned*>(P + o);
        const unsigned p2 = *reinterpret_cast<const unsigned*>(P + (size_t)S_ * D_ + o);
        const float sm = hi_f(pay[0]), sr = 1.0f / lo_f(pay[1]);
        hv[0] = rbf(lo_f(xv) + rbf(lo_f(p1) - lo_f(p2)));
        hv[1] = rbf(hi_f(xv) + rbf(hi_f(p1) - hi_f(p2)));
        lv[0] = rbf(rbf(hv[0] - sm) * sr); lv[1] = rbf(rbf(hv[1] - sm) * sr);
        sg = rbf(1.0f / (1.0f + __expf(-lo_f(pay[0]))));
    }
    u32x4 ga[12][2];
    for (unsigned spins = 0; spins < SPIN_CAP; ++spins) {
        const u32x2* gp = actg + (r & 7) * (F_ / 2) + w * 192 + q * 4;
#pragma unroll
        for (int i = 0; i < 12; ++i) {
            asm volatile("global_load_dwordx4 %0, %1, off offset:%2 sc1" : "=v"(ga[i][0]) : "v"(gp), "i"(i * 128) : "memory");
            asm volatile("global_load_dwordx4 %0, %1, off offset:%2 sc1" : "=v"(ga[i][1]) : "v"(gp), "i"(i * 128 + 16) : "memory");
        }
        asm volatile("s_waitcnt vmcnt(0)"
                     : "+v"(ga[0][0]), "+v"(ga[0][1]), "+v"(ga[1][0]), "+v"(ga[1][1]), "+v"(ga[2][0]), "+v"(ga[2][1]),
                       "+v"(ga[3][0]), "+v"(ga[3][1]), "+v"(ga[4][0]), "+v"(ga[4][1]), "+v"(ga[5][0]), "+v"(ga[5][1]) :: "memory");
        asm volatile(""
                     : "+v"(ga[6][0]), "+v"(ga[6][1]), "+v"(ga[7][0]), "+v"(ga[7][1]), "+v"(ga[8][0]), "+v"(ga[8][1]),
                       "+v"(ga[9][0]), "+v"(ga[9][1]), "+v"(ga[10][0]), "+v"(ga[10][1]), "+v"(ga[11][0]), "+v"(ga[11][1]) :: "memory");
        bool ok = true;
#pragma unroll
        for (int i = 0; i < 12; ++i)
            ok = ok && (ga[i][0][1] == ACT_TAG) && (ga[i][0][3] == ACT_TAG) && (ga[i][1][1] == ACT_TAG) && (ga[i][1][3] == ACT_TAG);
        if (__all(ok)) break;
        __builtin_amdgcn_s_sleep(4);
    }
    f32x4 a = {0.f, 0.f, 0.f, 0.f};
#pragma unroll
    for (int i = 0; i < 12; ++i) {
        const u32x4 lp = {ga[i][0][0], ga[i][0][2], ga[i][1][0], ga[i][1][2]};
        a = __builtin_amdgcn_mfma_f32_16x16x32_bf16(__builtin_bit_cast(s16x8, lp), tr_frag(&wimg[(w * 12 + i) * 1024], lane), a, 0, 0, 0);
    }
    if (q < 2) {
#pragma unroll
        for (int j = 0; j < 4; ++j) red[w][0][q * 4 + j][r] = a[j];
    }
    __syncthreads();
    if (w == 0) {
        for (unsigned spins = 0; spins < SPIN_CAP; ++spins) {
            unsigned v = 0;
            if (lane < 8) v = __hip_atomic_load(cnt + lane * CNT_STRIDE, __ATOMIC_RELAXED, __HIP_MEMORY_SCOPE_AGENT);
            v += dpp_u<0x124>(v); v += dpp_u<0x122>(v); v += dpp_u<0x121>(v); v += dpp_u<0x128>(v);
            if (__builtin_amdgcn_readfirstlane(v) >= 128u) break;
            __builtin_amdgcn_s_sleep(1);
        }
        const int et = lane & 7, ec = (lane >> 3) * 2;
        float o2[2];
#pragma unroll
        for (int e = 0; e < 2; ++e) {
            float y = red[0][0][et][ec + e] + red[1][0][et][ec + e] + red[2][0][et][ec + e] + red[3][0][et][ec + e];
            y = rbf(rbf(y) * sg);
            o2[e] = hv[e] + rbf(lv[e] + y);
        }
        st_sc1_b32(out + (size_t)my_row * D_ + c0 + ec, pack2(o2[0], o2[1]));
    }
}

extern "C" void kernel_launch(void* const* d_in, const int* in_sizes, int n_in,
                              void* d_out, int out_size, void* d_ws, size_t ws_size,
                              hipStream_t stream) {
    const u16* x   = (const u16*)d_in[0];
    const u16* Cre = (const u16*)d_in[1];
    const u16* Cim = (const u16*)d_in[2];
    const u16* Wre = (const u16*)d_in[3];
    const u16* Wim = (const u16*)d_in[4];
    const u16* wr  = (const u16*)d_in[6];
    const u16* wg  = (const u16*)d_in[7];
    const u16* wu  = (const u16*)d_in[8];
    const u16* wd  = (const u16*)d_in[9];
    u16* out = (u16*)d_out;

    char* ws = (char*)d_ws;
    u16* abT = (u16*)ws;
    u16* P = (u16*)(ws + (size_t)2 * D_ * S_ * 2);
    char* tail = ws + (size_t)4 * D_ * S_ * 2;
    u32x2* rg = (u32x2*)tail;
    u32x2* actg = (u32x2*)(tail + 16384);
    unsigned* cnt = (unsigned*)(tail + 16384 + 49152);

    (void)hipFuncSetAttribute((const void*)k_gemm1, hipFuncAttributeMaxDynamicSharedMemorySize, G1_LDS);
    (void)hipFuncSetAttribute((const void*)k_gemm2, hipFuncAttributeMaxDynamicSharedMemorySize, G2_LDS);

    k_gemm1<<<256, 512, G1_LDS, stream>>>(x, Wre, Wim, abT, (u32x4*)tail);
    k_gemm2<<<256, 512, G2_LDS, stream>>>(Cre, Cim, abT, P);
    k_tail<<<224, 256, 0, stream>>>(x, P, wr, wg, wu, wd, out, rg, actg, cnt);
}
```

```cpp
#include <hip/hip_runtime.h>
#include <hip/hip_bf16.h>

typedef unsigned short u16;
typedef __attribute__((ext_vector_type(8))) short s16x8;
typedef __attribute__((ext_vector_type(4))) float f32x4;
typedef __attribute__((ext_vector_type(4))) unsigned int u32x4;
typedef __attribute__((ext_vector_type(2))) unsigned int u32x2;

constexpr int S_ = 2048, D_ = 640, F_ = 1536, C_ = 8;

__device__ __forceinline__ float bf2f(u16 v) { return __uint_as_float(((unsigned)v) << 16); }
__device__ __forceinline__ u16 f2bf(float f) { __bf16 b = (__bf16)f; return __builtin_bit_cast(u16, b); }
__device__ __forceinline__ float rbf(float f) { return bf2f(f2bf(f)); }
__device__ __forceinline__ float lo_f(unsigned w) { return __uint_as_float(w << 16); }
__device__ __forceinline__ float hi_f(unsigned w) { return __uint_as_float(w & 0xffff0000u); }
__device__ __forceinline__ unsigned pack2(float a, float b) { return (unsigned)f2bf(a) | ((unsigned)f2bf(b) << 16); }

typedef __attribute__((ext_vector_type(2))) float f32x2;
typedef __attribute__((ext_vector_type(2))) __bf16 bf16x2_t;
__device__ __forceinline__ unsigned cvt2(f32x2 v) { return __builtin_bit_cast(unsigned, __builtin_convertvector(v, bf16x2_t)); }
__device__ __forceinline__ f32x2 unp2(unsigned w) { f32x2 r; r[0] = lo_f(w); r[1] = hi_f(w); return r; }
__device__ __forceinline__ f32x2 rbf2(f32x2 v) { return unp2(cvt2(v)); }

template <int CTRL> __device__ __forceinline__ unsigned dpp_u(unsigned v) {
    return (unsigned)__builtin_amdgcn_update_dpp(0, (int)v, CTRL, 0xf, 0xf, false);
}
template <int CTRL> __device__ __forceinline__ float dpp_f(float v) { return __uint_as_float(dpp_u<CTRL>(__float_as_uint(v))); }
__device__ __forceinline__ float red16_sum(float v) {
    v += dpp_f<0x128>(v); v += dpp_f<0x124>(v); v += dpp_f<0x122>(v); v += dpp_f<0x121>(v);
    return v;
}

__device__ __forceinline__ void ln_finish(float sum_sq_dev, float mean, float& m_bf, float& rinv) {
    float v = rbf(sum_sq_dev * (1.0f / D_));
    float den = rbf(sqrtf(rbf(v + 1e-5f)));
    m_bf = rbf(mean);
    rinv = 1.0f / den;
}

__device__ __forceinline__ void dma16(const u16* base, unsigned voff, unsigned ldsaddr) {
    unsigned keep;
    asm volatile("s_mov_b32 %0, m0\n\ts_mov_b32 m0, %3\n\ts_nop 0\n\tglobal_load_lds_dwordx4 %1, %2\n\ts_mov_b32 m0, %0"
                 : "=&s"(keep) : "v"(voff), "s"(base), "s"(ldsaddr) : "memory");
}
__device__ __forceinline__ unsigned l2_touch(const u16* base, unsigned voff) {
    unsigned d;
    asm volatile("global_load_dword %0, %1, %2" : "=v"(d) : "v"(voff), "s"(base) : "memory");
    return d;
}
#define l2_touch_done5(a, b, c, d, e) asm volatile("s_waitcnt vmcnt(0)" : "+v"(a), "+v"(b), "+v"(c), "+v"(d), "+v"(e) :: "memory")
#define l2_touch_done1(a) asm volatile("s_waitcnt vmcnt(0)" : "+v"(a) :: "memory")
#define LGKM_BAR() asm volatile("s_waitcnt lgkmcnt(0)\n\ts_barrier" ::: "memory")
#define VM_BAR(N) asm volatile("s_waitcnt vmcnt(" #N ")\n\ts_barrier" ::: "memory")
#define FRAG_BAR(A, B)                                                                              \
    asm volatile("s_waitcnt lgkmcnt(0)\n\ts_barrier"                                                \
                 : "+v"(A[0]), "+v"(A[1]), "+v"(A[2]), "+v"(A[3]), "+v"(B[0]), "+v"(B[1]), "+v"(B[2]), \
                   "+v"(B[3]), "+v"(B[4]), "+v"(B[5]), "+v"(B[6]), "+v"(B[7]), "+v"(B[8]), "+v"(B[9]) \
                 :: "memory")
#define INTERLEAVE_14_20()                                                              \
    do {                                                                                \
        _Pragma("unroll") for (int i_ = 0; i_ < 14; ++i_) {                             \
            __builtin_amdgcn_sched_group_barrier(0x100, 1, 0);                          \
            __builtin_amdgcn_sched_group_barrier(0x008, 1, 0);                          \
        }                                                                               \
        __builtin_amdgcn_sched_group_barrier(0x008, 6, 0);                              \
        __builtin_amdgcn_sched_barrier(0);                                              \
    } while (0)

constexpr int G1_XN = 64 * 1280;
constexpr int G1_SLOT = 160 * 128;
constexpr int G1_LDS = G1_XN + 4 * G1_SLOT;

__global__ __launch_bounds__(512, 2) void k_gemm1(const u16* __restrict__ x, const u16* __restrict__ Wre,
                                                  const u16* __restrict__ Wim, u16* __restrict__ abT,
                                                  u32x4* __restrict__ clr) {
    extern __shared__ __attribute__((aligned(16))) char smem[];
    const int bid = blockIdx.x, xcd = bid & 7, loc = bid >> 3;
    if (bid < 9) {
        const int i = bid * 512 + threadIdx.x;
        if (i < (16384 + 49152 + 4096) / 16) clr[i] = u32x4{0u, 0u, 0u, 0u};
    }
    const int st = (xcd >> 1) * 8 + (loc & 7);
    const int et = (xcd & 1) * 4 + (loc >> 3);
    const int s0 = st * 64, e0 = et * 160;
    const u16* W = (et < 4) ? (Wre + (size_t)e0 * D_) : (Wim + (size_t)(e0 - D_) * D_);
    const int tid = threadIdx.x, lane = tid & 63;
    const int w = __builtin_amdgcn_readfirstlane(tid >> 6);
    const int r = lane & 15, q = lane >> 4;
    const unsigned smem_base = (unsigned)(size_t)(smem);

    u32x4 xv[2][5];
    {
        const unsigned xo = (unsigned)(((s0 + w * 8 + q) * D_ + r * 8) * 2);
#pragma unroll
        for (int i = 0; i < 2; ++i)
#pragma unroll
            for (int c = 0; c < 5; ++c)
                asm volatile("global_load_dwordx4 %0, %1, %2" : "=v"(xv[i][c]) : "v"(xo + (unsigned)(i * 4 * D_ * 2 + c * 256)), "s"(x) : "memory");
    }
    unsigned wv[5];
    const int p = w - 4;
#pragma unroll
    for (int i = 0; i < 5; ++i) {
        const int row = (p & 3) * 40 + i * 8 + (lane >> 3);
        wv[i] = (unsigned)(row * D_ * 2 + (((lane & 7) ^ (row & 7)) * 16));
    }
    const unsigned wl = smem_base + G1_XN + (unsigned)((p & 3) * 5120);
#define G1_ISSUE(K)                                                                     \
    do {                                                                                \
        _Pragma("unroll") for (int i_ = 0; i_ < 5; ++i_)                                \
            dma16(W + (K) * 64, wv[i_], wl + (unsigned)(((K) & 3) * G1_SLOT + i_ * 1024)); \
    } while (0)
    if (w >= 4) { G1_ISSUE(0); G1_ISSUE(1); G1_ISSUE(2); G1_ISSUE(3); }
    unsigned pf = 0;
    if (w < 4) {
        int li = w * 64 + lane;
        li = li < 200 ? li : 199;
        pf = l2_touch(W + (size_t)((loc & 7) * 20) * D_, (unsigned)((li % 20) * D_ * 2 + (li / 20) * 128));
    }

    if (w >= 4) asm volatile("s_waitcnt vmcnt(20)" ::: "memory");
    else asm volatile("s_waitcnt vmcnt(1)" ::: "memory");
    asm volatile("" : "+v"(xv[0][0]), "+v"(xv[0][1]), "+v"(xv[0][2]), "+v"(xv[0][3]), "+v"(xv[0][4]),
                      "+v"(xv[1][0]), "+v"(xv[1][1]), "+v"(xv[1][2]), "+v"(xv[1][3]), "+v"(xv[1][4]) :: "memory");
#pragma unroll
    for (int i = 0; i < 2; ++i) {
        f32x2 v[20];
        f32x2 s2 = {0.f, 0.f};
#pragma unroll
        for (int c = 0; c < 5; ++c)
#pragma unroll
            for (int j = 0; j < 4; ++j) { v[c * 4 + j] = unp2(xv[i][c][j]); s2 += v[c * 4 + j]; }
        const float mean = red16_sum(s2[0] + s2[1]) * (1.0f / D_);
        f32x2 q2 = {0.f, 0.f};
#pragma unroll
        for (int j = 0; j < 20; ++j) { const f32x2 d = v[j] - mean; q2 += d * d; }
        float m_bf, rinv;
        ln_finish(red16_sum(q2[0] + q2[1]), mean, m_bf, rinv);
        const int row = w * 8 + i * 4 + q;
#pragma unroll
        for (int c = 0; c < 5; ++c) {
            u32x4 o;
#pragma unroll
            for (int j = 0; j < 4; ++j) o[j] = cvt2(rbf2(v[c * 4 + j] - m_bf) * rinv);
            const int ch = (c * 16 + r) ^ (row & 15);
            *reinterpret_cast<u32x4*>(smem + row * 1280 + ch * 16) = o;
        }
    }

    if (w >= 4) {
        asm volatile("s_waitcnt vmcnt(15) lgkmcnt(0)\n\ts_barrier" ::: "memory");
#pragma unroll
        for (int k = 0; k < 10; ++k) {
            if (k <= 6) VM_BAR(10); else VM_BAR(0);
            if (k + 4 < 10) G1_ISSUE(k + 4);
        }
        return;
    }
    l2_touch_done1(pf);
    const int ws = w & 1, we = w >> 1;
    f32x4 acc[2][5];
#pragma unroll
    for (int m = 0; m < 2; ++m)
#pragma unroll
        for (int n = 0; n < 5; ++n) acc[m][n] = f32x4{0.f, 0.f, 0.f, 0.f};
    const char* xbase = smem + (ws * 32 + r) * 1280;
    const char* wbase = smem + G1_XN + (we * 80 + r) * 128;
    s16x8 xf[2][4], wf[2][10];
#define G1_READ(B, K)                                                                   \
    do {                                                                                \
        _Pragma("unroll") for (int h_ = 0; h_ < 2; ++h_) {                              \
            _Pragma("unroll") for (int m_ = 0; m_ < 2; ++m_)                            \
                xf[B][m_ * 2 + h_] = *reinterpret_cast<const s16x8*>(xbase + m_ * 16 * 1280 + ((((K) * 8 + h_ * 4 + q) ^ r) * 16)); \
            _Pragma("unroll") for (int n_ = 0; n_ < 5; ++n_)                            \
                wf[B][n_ * 2 + h_] = *reinterpret_cast<const s16x8*>(wbase + ((K) & 3) * G1_SLOT + n_ * 2048 + (((h_ * 4 + q) ^ (r & 7)) * 16)); \
        }                                                                               \
    } while (0)
    LGKM_BAR();
    G1_READ(0, 0);
#pragma unroll
    for (int k = 0; k < 10; ++k) {
        FRAG_BAR(xf[k & 1], wf[k & 1]);
        if (k + 1 < 10) G1_READ((k + 1) & 1, k + 1);
#pragma unroll
        for (int h = 0; h < 2; ++h)
#pragma unroll
            for (int m = 0; m < 2; ++m)
#pragma unroll
                for (int n = 0; n < 5; ++n)
                    acc[m][n] = __builtin_amdgcn_mfma_f32_16x16x32_bf16(xf[k & 1][m * 2 + h], wf[k & 1][n * 2 + h], acc[m][n], 0, 0, 0);
        if (k + 1 < 10) INTERLEAVE_14_20();
    }
    char* img = smem + G1_XN + 2 * G1_SLOT + w * 5120;
#pragma unroll
    for (int m = 0; m < 2; ++m)
#pragma unroll
        for (int n = 0; n < 5; ++n) {
            u32x2 o;
            o[0] = pack2(acc[m][n][0], acc[m][n][1]);
            o[1] = pack2(acc[m][n][2], acc[m][n][3]);
            *reinterpret_cast<u32x2*>(img + (n * 16 + r) * 64 + m * 32 + q * 8) = o;
        }
    u16* dst = abT + (size_t)(e0 + we * 80) * S_ + s0 + ws * 32;
#pragma unroll
    for (int i = 0; i < 5; ++i) {
        const int c = i * 64 + lane;
        const u32x4 v = *reinterpret_cast<const u32x4*>(img + c * 16);
        *reinterpret_cast<u32x4*>(dst + (size_t)(c >> 2) * S_ + (c & 3) * 8) = v;
    }
}

#define G2_ROT 0
#define G2_T2 8
#define G2_T3 16
constexpr int G2_SLOT = (128 + 80) * 128;
constexpr int G2_NST = 6;
constexpr int G2_LDS = G2_NST * G2_SLOT;

__global__ __launch_bounds__(512, 2) void k_gemm2(const u16* __restrict__ Cre, const u16* __restrict__ Cim,
                                                  const u16* __restrict__ abT, u16* __restrict__ P,
                                                  const u16* __restrict__ wg, const u16* __restrict__ wu,
                                                  const u16* __restrict__ wd) {
    extern __shared__ __attribute__((aligned(16))) char smem[];
    const int bid = blockIdx.x, xcd = bid & 7, loc = bid >> 3;
    const int part = xcd & 1;
    const int tp = (xcd >> 1) * 4 + (loc >> 3);
    const int ct = loc & 7;
    const int t0 = tp * 128, e0 = ct * 80;
    const int tid = threadIdx.x, lane = tid & 63;
    const int w = __builtin_amdgcn_readfirstlane(tid >> 6);
    const int r = lane & 15, q = lane >> 4;
    const unsigned smem_base = (unsigned)(size_t)(smem);

    if (w >= 4) {
        const int p = w - 4;
        const u16* Cb = (part ? Cim : Cre) + (size_t)t0 * S_;
        const u16* Bb = abT + (size_t)(part * D_ + e0) * S_;
        unsigned av[4], bv[3];
#pragma unroll
        for (int i = 0; i < 4; ++i) {
            const int row = p * 32 + i * 8 + (lane >> 3);
            av[i] = (unsigned)(row * S_ * 2 + (((lane & 7) ^ (row & 7)) * 16));
        }
#pragma unroll
        for (int i = 0; i < 3; ++i) {
            const int o = p * 2560 + i * 1024 + lane * 16;
            const int row = o >> 7, cs = (o >> 4) & 7;
            bv[i] = (unsigned)(row * S_ * 2 + ((cs ^ (row & 7)) * 16));
        }
        const int rot = G2_ROT;
        const unsigned la = smem_base + (unsigned)(p * 4096);
        const unsigned lb = smem_base + 16384u + (unsigned)(p * 2560);
#define G2_ISSUE(K, SO)                                                                 \
        do {                                                                            \
            const int kk_ = ((K) + rot) & 31;                                           \
            const u16* cb_ = Cb + kk_ * 64;                                             \
            const u16* bb_ = Bb + kk_ * 64;                                             \
            _Pragma("unroll") for (int i_ = 0; i_ < 4; ++i_) dma16(cb_, av[i_], la + (SO) + i_ * 1024u); \
            dma16(bb_, bv[0], lb + (SO));                                               \
            dma16(bb_, bv[1], lb + (SO) + 1024u);                                       \
            if (lane < 32) dma16(bb_, bv[2], lb + (SO) + 2048u);                        \
        } while (0)
#pragma unroll
        for (int k = 0; k < G2_NST; ++k) G2_ISSUE(k, (unsigned)(k * G2_SLOT));
        VM_BAR(35);
        unsigned so = 0;
        for (int k = 0; k < 32; ++k) {
            if (k <= 26) VM_BAR(28); else VM_BAR(0);
            if (k + G2_NST < 32) G2_ISSUE(k + G2_NST, so);
            so = (so == (unsigned)((G2_NST - 1) * G2_SLOT)) ? 0u : so + (unsigned)G2_SLOT;
        }
        return;
    }
    const u16 *pfp0, *pfp1;
    {
        const int l0 = w * 9 + (lane >> 3), l1 = w * 9 + 8;
        const u16* Cp = (part ? Cim : Cre) + (size_t)(t0 + ct * 16) * S_ + (lane & 7) * 64;
        const u16* Bp = abT + (size_t)(part * D_ + e0 + (loc >> 3) * 20) * S_ + (lane & 7) * 64;
        pfp0 = (l0 < 16) ? (Cp + (size_t)l0 * S_) : (Bp + (size_t)(l0 - 16) * S_);
        pfp1 = (l1 < 16) ? (Cp + (size_t)l1 * S_) : (Bp + (size_t)(l1 - 16) * S_);
    }
    unsigned pfd = 0;
#define G2_TOUCH(J)                                                                                     \
    do {                                                                                                \
        asm volatile("global_load_dword %0, %1, off" : "+v"(pfd) : "v"(pfp0 + (J) * 512) : "memory");   \
        asm volatile("global_load_dword %0, %1, off" : "+v"(pfd) : "v"(pfp1 + (J) * 512) : "memory");   \
    } while (0)
    G2_TOUCH(0); G2_TOUCH(1);
    f32x4 acc[5][2];
#pragma unroll
    for (int n = 0; n < 5; ++n)
#pragma unroll
        for (int m = 0; m < 2; ++m) acc[n][m] = f32x4{0.f, 0.f, 0.f, 0.f};
    const int rd0 = r * 128 + ((q ^ (r & 7)) * 16);
    const int rd1 = r * 128 + (((q + 4) ^ (r & 7)) * 16);
    const int aw = w * 4096;
    s16x8 cf[2][4], ef[2][10];
#define G2_READ(B, SO)                                                                  \
    do {                                                                                \
        const char* s0_ = smem + (SO) + rd0;                                            \
        const char* s1_ = smem + (SO) + rd1;                                            \
        _Pragma("unroll") for (int m_ = 0; m_ < 2; ++m_) {                              \
            cf[B][m_ * 2 + 0] = *reinterpret_cast<const s16x8*>(s0_ + aw + m_ * 2048);  \
            cf[B][m_ * 2 + 1] = *reinterpret_cast<const s16x8*>(s1_ + aw + m_ * 2048);  \
        }                                                                               \
        _Pragma("unroll") for (int n_ = 0; n_ < 5; ++n_) {                              \
            ef[B][n_ * 2 + 0] = *reinterpret_cast<const s16x8*>(s0_ + 16384 + n_ * 2048); \
            ef[B][n_ * 2 + 1] = *reinterpret_cast<const s16x8*>(s1_ + 16384 + n_ * 2048); \
        }                                                                               \
    } while (0)
#define G2_MMA(B)                                                                       \
    do {                                                                                \
        _Pragma("unroll") for (int h_ = 0; h_ < 2; ++h_)                                \
        _Pragma("unroll") for (int n_ = 0; n_ < 5; ++n_)                                \
        _Pragma("unroll") for (int m_ = 0; m_ < 2; ++m_)                                \
            acc[n_][m_] = __builtin_amdgcn_mfma_f32_16x16x32_bf16(ef[B][n_ * 2 + h_], cf[B][m_ * 2 + h_], acc[n_][m_], 0, 0, 0); \
    } while (0)
    LGKM_BAR();
    G2_READ(0, 0);
    int so = G2_SLOT;
    for (int k = 0; k < 32; k += 2) {
        FRAG_BAR(cf[0], ef[0]);
        if (k == G2_T2) G2_TOUCH(2);
        if (k == G2_T3) G2_TOUCH(3);
        if (k == 26) {
            const int li = (bid * 4 + w) * 64 + lane;
            const int mi = li / 15360;
            const u16* wp = (mi == 0 ? wg : (mi == 1 ? wu : wd)) + (size_t)(mi < 3 ? li - mi * 15360 : lane) * 64;
            asm volatile("global_load_dword %0, %1, off" : "+v"(pfd) : "v"(wp) : "memory");
        }
        G2_READ(1, so);
        so = (so == (G2_NST - 1) * G2_SLOT) ? 0 : so + G2_SLOT;
        G2_MMA(0);
        INTERLEAVE_14_20();
        FRAG_BAR(cf[1], ef[1]);
        G2_READ(0, so);
        so = (so == (G2_NST - 1) * G2_SLOT) ? 0 : so + G2_SLOT;
        G2_MMA(1);
        INTERLEAVE_14_20();
    }
    l2_touch_done1(pfd);
    char* img = smem + 3 * G2_SLOT + w * 5120;
#pragma unroll
    for (int m = 0; m < 2; ++m)
#pragma unroll
        for (int n = 0; n < 5; ++n) {
            u32x2 o;
            o[0] = pack2(acc[n][m][0], acc[n][m][1]);
            o[1] = pack2(acc[n][m][2], acc[n][m][3]);
            *reinterpret_cast<u32x2*>(img + (m * 16 + r) * 160 + n * 32 + q * 8) = o;
        }
    u16* dst = P + (size_t)part * S_ * D_ + (size_t)(t0 + w * 32) * D_ + e0;
#pragma unroll
    for (int i = 0; i < 5; ++i) {
        const int c = i * 64 + lane;
        const u32x4 v = *reinterpret_cast<const u32x4*>(img + c * 16);
        const int row = c / 10;
        *reinterpret_cast<u32x4*>(dst + (size_t)row * D_ + (c - row * 10) * 8) = v;
    }
}

__device__ __forceinline__ unsigned topk_key(float v, int idx) {
    unsigned b = __float_as_uint(v) >> 16;
    b = (b & 0x8000u) ? (~b & 0xffffu) : (b | 0x8000u);
    return (b << 11) | (unsigned)(S_ - 1 - idx);
}
__device__ __forceinline__ unsigned umax_(unsigned a, unsigned b) { return a > b ? a : b; }
__device__ __forceinline__ unsigned wave_max_u32(unsigned v) {
    v = umax_(v, dpp_u<0x128>(v)); v = umax_(v, dpp_u<0x124>(v)); v = umax_(v, dpp_u<0x122>(v)); v = umax_(v, dpp_u<0x121>(v));
    const unsigned a = __builtin_amdgcn_readlane(v, 0), b = __builtin_amdgcn_readlane(v, 16);
    const unsigned c = __builtin_amdgcn_readlane(v, 32), d = __builtin_amdgcn_readlane(v, 48);
    return umax_(umax_(a, b), umax_(c, d));
}
typedef __attribute__((ext_vector_type(4))) short s16x4;
typedef __attribute__((address_space(3))) s16x4* lds_s16x4_p;
__device__ __forceinline__ s16x8 tr_frag(const char* img, int lane) {
    const int i = lane & 15, g = lane >> 4;
    const char* a = img + (g * 8 + (i >> 2)) * 32 + (i & 3) * 8;
    const s16x4 lo = __builtin_amdgcn_ds_read_tr16_b64_v4i16((lds_s16x4_p)(a));
    const s16x4 hi = __builtin_amdgcn_ds_read_tr16_b64_v4i16((lds_s16x4_p)(a + 128));
    return s16x8{lo[0], lo[1], lo[2], lo[3], hi[0], hi[1], hi[2], hi[3]};
}

constexpr int CNT_STRIDE = 64;
constexpr unsigned RG_TAG = 0x5AA5u;
constexpr unsigned ACT_TAG = 0x7C3E91B5u;
constexpr unsigned SPIN_CAP = 1u << 20;
__device__ __forceinline__ void st_sc1_b128(void* p, u32x4 v) {
    asm volatile("global_store_dwordx4 %0, %1, off sc1\n\ts_nop 1" :: "v"(p), "v"(v) : "memory");
}
__device__ __forceinline__ void st_sc1_b64(void* p, u32x2 v) {
    asm volatile("global_store_dwordx2 %0, %1, off sc1\n\ts_nop 1" :: "v"(p), "v"(v) : "memory");
}
__device__ __forceinline__ void st_sc1_b32(void* p, unsigned v) {
    asm volatile("global_store_dword %0, %1, off sc1\n\ts_nop 1" :: "v"(p), "v"(v) : "memory");
}

__device__ __forceinline__ int route_top8(const u32x2* rg, unsigned* cand, u32x2* lrg, int w, int lane, u32x2& pay) {
    u32x4 g[4];
    for (unsigned spins = 0; spins < SPIN_CAP; ++spins) {
#pragma unroll
        for (int i = 0; i < 4; ++i)
            asm volatile("global_load_dwordx4 %0, %1, %2 sc1" : "=v"(g[i]) : "v"((unsigned)((w * 256 + i * 64 + lane) * 16)), "s"(rg) : "memory");
        asm volatile("s_waitcnt vmcnt(0)" : "+v"(g[0]), "+v"(g[1]), "+v"(g[2]), "+v"(g[3]) :: "memory");
        bool ok = true;
#pragma unroll
        for (int i = 0; i < 4; ++i) ok = ok && ((g[i][1] >> 16) == RG_TAG) && ((g[i][3] >> 16) == RG_TAG);
        if (__all(ok)) break;
        __builtin_amdgcn_s_sleep(2);
    }
    unsigned key[8];
#pragma unroll
    for (int i = 0; i < 4; ++i) {
        const int idx = (w * 256 + i * 64 + lane) * 2;
        key[2 * i] = topk_key(lo_f(g[i][0]), idx);
        key[2 * i + 1] = topk_key(lo_f(g[i][2]), idx + 1);
        *reinterpret_cast<u32x4*>(lrg + idx) = g[i];
    }
    unsigned prev = 1u << 27, my_key = 0;
#pragma unroll
    for (int t = 0; t < 8; ++t) {
        unsigned m = 0;
#pragma unroll
        for (int i = 0; i < 8; ++i) m = umax_(m, key[i] - prev);
        prev += wave_max_u32(m);
        my_key = (lane == t) ? prev : my_key;
    }
    if (lane < 8) cand[w * 8 + lane] = my_key;
    __syncthreads();
    const unsigned ck = cand[lane & 31];
    prev = 1u << 27; my_key = 0;
#pragma unroll
    for (int t = 0; t < 8; ++t) {
        prev += wave_max_u32(ck - prev);
        my_key = ((lane & 7) == t) ? prev : my_key;
    }
    const int row = (S_ - 1 - (int)(my_key & 2047u)) & 2047;
    pay = lrg[row];
    return row;
}

__global__ __launch_bounds__(256) void k_tail(const u16* __restrict__ x, const u16* __restrict__ P,
                                              const u16* __restrict__ wr, const u16* __restrict__ wg,
                                              const u16* __restrict__ wu, const u16* __restrict__ wd,
                                              u16* out, u32x2* rg, u32x2* actg, unsigned* cnt) {
    __shared__ __attribute__((aligned(16))) char wimg[48 * 1024];
    __shared__ __attribute__((aligned(16))) u32x2 lrg[2048];
    __shared__ __attribute__((aligned(16))) char tok[4 * 8 * 784];
    __shared__ float red[4][2][8][16];
    __shared__ unsigned cand[32];
    __shared__ unsigned arrived;
    const int bid = blockIdx.x;
    const int tid = threadIdx.x, lane = tid & 63, r = lane & 15, q = lane >> 4;
    const int w = __builtin_amdgcn_readfirstlane(tid >> 6);
    const unsigned l0 = (unsigned)(size_t)(&wimg[0]);

    if (bid >= 128) {
        const int ju = bid - 128;
        const int c0 = ((ju & 7) * 12 + (ju >> 3)) * 16;
        {
            const unsigned voff = (unsigned)(((w * 160 + (lane >> 1)) * F_ + c0 + (lane & 1) * 8) * 2);
#pragma unroll
            for (int i = 0; i < 5; ++i) {
                dma16(wg, voff + (unsigned)(i * 32 * F_ * 2), l0 + (unsigned)((w * 5 + i) * 1024));
                dma16(wu, voff + (unsigned)(i * 32 * F_ * 2), l0 + (unsigned)(20480 + (w * 5 + i) * 1024));
            }
        }
        u32x2 pay;
        const int my_row = route_top8(rg, cand, lrg, w, lane, pay);
        {
            u32x4 xv[3], p1[3], p2[3];
            int tk[3], ch[3];
#pragma unroll
            for (int i = 0; i < 3; ++i) {
                int c = i * 64 + lane; c = c < 160 ? c : 159;
                tk[i] = 2 * w + (c >= 80 ? 1 : 0); ch[i] = c >= 80 ? c - 80 : c;
                const int rw = __shfl(my_row, tk[i]);
                const size_t o = (size_t)rw * D_ + ch[i] * 8;
                xv[i] = *reinterpret_cast<const u32x4*>(x + o);
                p1[i] = *reinterpret_cast<const u32x4*>(P + o);
                p2[i] = *reinterpret_cast<const u32x4*>(P + (size_t)S_ * D_ + o);
            }
#pragma unroll
            for (int i = 0; i < 3; ++i) {
                const float sm = hi_f(__shfl(pay[0], tk[i])), sr = 1.0f / lo_f(__shfl(pay[1], tk[i]));
                u32x4 lp;
#pragma unroll
                for (int j = 0; j < 4; ++j) {
                    const f32x2 hh = rbf2(unp2(xv[i][j]) + rbf2(unp2(p1[i][j]) - unp2(p2[i][j])));
                    lp[j] = cvt2(rbf2(hh - sm) * sr);
                }
                *reinterpret_cast<u32x4*>(tok + tk[i] * 1296 + ch[i] * 16) = lp;
            }
        }
        __syncthreads();
        f32x4 ag = {0.f, 0.f, 0.f, 0.f}, au = {0.f, 0.f, 0.f, 0.f};
#pragma unroll
        for (int i = 0; i < 5; ++i) {
            const s16x8 af = *reinterpret_cast<const s16x8*>(tok + (r & 7) * 1296 + ((w * 5 + i) * 4 + q) * 16);
            ag = __builtin_amdgcn_mfma_f32_16x16x32_bf16(af, tr_frag(&wimg[(w * 5 + i) * 1024], lane), ag, 0, 0, 0);
            au = __builtin_amdgcn_mfma_f32_16x16x32_bf16(af, tr_frag(&wimg[20480 + (w * 5 + i) * 1024], lane), au, 0, 0, 0);
        }
        if (q < 2) {
#pragma unroll
            for (int j = 0; j < 4; ++j) { red[w][0][q * 4 + j][r] = ag[j]; red[w][1][q * 4 + j][r] = au[j]; }
        }
        __syncthreads();
        if (tid < 64) {
            const int t = tid >> 3, c = (tid & 7) * 2;
            float o2[2];
#pragma unroll
            for (int e = 0; e < 2; ++e) {
                float g = red[0][0][t][c + e] + red[1][0][t][c + e] + red[2][0][t][c + e] + red[3][0][t][c + e];
                float u = red[0][1][t][c + e] + red[1][1][t][c + e] + red[2][1][t][c + e] + red[3][1][t][c + e];
                g = rbf(g); u = rbf(u);
                o2[e] = rbf(g / (1.0f + __expf(-g))) * u;
            }
            u32x2 gv; gv[0] = pack2(o2[0], o2[1]); gv[1] = ACT_TAG;
            st_sc1_b64(actg + t * (F_ / 2) + (c0 + c) / 2, gv);
        }
        return;
    }

    if (tid == 0) arrived = 0u;
    __syncthreads();
    {
        const int row = bid * 16 + w * 4 + q;
        const size_t ro = (size_t)row * D_;
        u32x4 xv[5], p1[5], p2[5], wv[5];
#pragma unroll
        for (int c = 0; c < 5; ++c) {
            const int o = (c * 16 + r) * 8;
            xv[c] = *reinterpret_cast<const u32x4*>(x + ro + o);
            p1[c] = *reinterpret_cast<const u32x4*>(P + ro + o);
            p2[c] = *reinterpret_cast<const u32x4*>(P + (size_t)S_ * D_ + ro + o);
            wv[c] = *reinterpret_cast<const u32x4*>(wr + o);
        }
        f32x2 h[20];
        f32x2 s2 = {0.f, 0.f};
#pragma unroll
        for (int c = 0; c < 5; ++c)
#pragma unroll
            for (int j = 0; j < 4; ++j) {
                h[c * 4 + j] = rbf2(unp2(xv[c][j]) + rbf2(unp2(p1[c][j]) - unp2(p2[c][j])));
                s2 += h[c * 4 + j];
            }
        const float mean = red16_sum(s2[0] + s2[1]) * (1.0f / D_);
        f32x2 q2 = {0.f, 0.f};
#pragma unroll
        for (int i = 0; i < 20; ++i) { const f32x2 d = h[i] - mean; q2 += d * d; }
        const float sq = red16_sum(q2[0] + q2[1]);
        const float m_bf = rbf(mean);
        const float den = rbf(sqrtf(rbf(rbf(sq * (1.0f / D_)) + 1e-5f)));
        const float rinv = 1.0f / den;
        f32x2 d2 = {0.f, 0.f};
        u32x4 o[5];
#pragma unroll
        for (int c = 0; c < 5; ++c)
#pragma unroll
            for (int j = 0; j < 4; ++j) {
                const f32x2 a = rbf2(rbf2(h[c * 4 + j] - m_bf) * rinv);
                d2 += a * unp2(wv[c][j]);
                o[c][j] = cvt2(h[c * 4 + j] + a);
            }
        const float dot = red16_sum(d2[0] + d2[1]);
        if (r == 0) {
            u32x2 gv; gv[0] = pack2(dot, m_bf); gv[1] = (unsigned)f2bf(den) | (RG_TAG << 16);
            st_sc1_b64(rg + row, gv);
        }
#pragma unroll
        for (int c = 0; c < 5; ++c) st_sc1_b128(out + ro + (c * 16 + r) * 8, o[c]);
        asm volatile("s_waitcnt vmcnt(0)" ::: "memory");
        if (lane == 0) {
            const unsigned old = __hip_atomic_fetch_add(&arrived, 1u, __ATOMIC_RELAXED, __HIP_MEMORY_SCOPE_WORKGROUP);
            if (old == 3u) __hip_atomic_fetch_add(cnt + (bid & 7) * CNT_STRIDE, 1u, __ATOMIC_RELAXED, __HIP_MEMORY_SCOPE_AGENT);
        }
    }
    if (bid >= 40) return;

    const int c0 = ((bid & 7) * 5 + (bid >> 3)) * 16;
    {
        const unsigned voff = (unsigned)(((w * 384 + (lane >> 1)) * D_ + c0 + (lane & 1) * 8) * 2);
#pragma unroll
        for (int i = 0; i < 12; ++i) dma16(wd, voff + (unsigned)(i * 32 * D_ * 2), l0 + (unsigned)((w * 12 + i) * 1024));
    }
    u32x2 pay;
    const int my_row = route_top8(rg, cand, lrg, w, lane, pay);
    float hv[2], lv[2], sg;
    {
        const size_t o = (size_t)my_row * D_ + c0 + (lane >> 3) * 2;
        const unsigned xv = *reinterpret_cast<const unsigned*>(x + o);
        const unsigned p1 = *reinterpret_cast<const unsigned*>(P + o);
        const unsigned p2 = *reinterpret_cast<const unsigned*>(P + (size_t)S_ * D_ + o);
        const float sm = hi_f(pay[0]), sr = 1.0f / lo_f(pay[1]);
        hv[0] = rbf(lo_f(xv) + rbf(lo_f(p1) - lo_f(p2)));
        hv[1] = rbf(hi_f(xv) + rbf(hi_f(p1) - hi_f(p2)));
        lv[0] = rbf(rbf(hv[0] - sm) * sr); lv[1] = rbf(rbf(hv[1] - sm) * sr);
        sg = rbf(1.0f / (1.0f + __expf(-lo_f(pay[0]))));
    }
    if (w == 0) {
        for (unsigned spins = 0; spins < SPIN_CAP; ++spins) {
            unsigned v = 0;
            if (lane < 8) v = __hip_atomic_load(cnt + lane * CNT_STRIDE, __ATOMIC_RELAXED, __HIP_MEMORY_SCOPE_AGENT);
            v += dpp_u<0x0B1>(v); v += dpp_u<0x04E>(v); v += dpp_u<0x141>(v);
            if (__builtin_amdgcn_readfirstlane(v) >= 128u) break;
            __builtin_amdgcn_s_sleep(2);
        }
    }
    for (unsigned spins = 0; spins < SPIN_CAP; ++spins) {
        u32x2 g1;
        asm volatile("global_load_dwordx2 %0, %1, off sc1\n\ts_waitcnt vmcnt(0)" : "=v"(g1)
                     : "v"(actg + 7 * (F_ / 2) + w * 192 + (lane < 24 ? lane : 23) * 8 + 7) : "memory");
        if (__all(g1[1] == ACT_TAG)) break;
        __builtin_amdgcn_s_sleep(1);
    }
    u32x4 ga[12];
    for (unsigned spins = 0; spins < SPIN_CAP; ++spins) {
#pragma unroll
        for (int i = 0; i < 12; ++i) {
            const int c = i * 64 + lane, t = c / 96;
            asm volatile("global_load_dwordx4 %0, %1, off sc1" : "=v"(ga[i]) : "v"(actg + t * (F_ / 2) + w * 192 + (c - t * 96) * 2) : "memory");
        }
        asm volatile("s_waitcnt vmcnt(0)"
                     : "+v"(ga[0]), "+v"(ga[1]), "+v"(ga[2]), "+v"(ga[3]), "+v"(ga[4]), "+v"(ga[5]),
                       "+v"(ga[6]), "+v"(ga[7]), "+v"(ga[8]), "+v"(ga[9]), "+v"(ga[10]), "+v"(ga[11]) :: "memory");
        bool ok = true;
#pragma unroll
        for (int i = 0; i < 12; ++i) ok = ok && (ga[i][1] == ACT_TAG) && (ga[i][3] == ACT_TAG);
        if (__all(ok)) break;
        __builtin_amdgcn_s_sleep(1);
    }
    char* aimg = tok + w * (8 * 784);
#pragma unroll
    for (int i = 0; i < 12; ++i) {
        const int c = i * 64 + lane, t = c / 96;
        u32x2 v; v[0] = ga[i][0]; v[1] = ga[i][2];
        *reinterpret_cast<u32x2*>(aimg + t * 784 + (c - t * 96) * 8) = v;
    }
    f32x4 a = {0.f, 0.f, 0.f, 0.f};
#pragma unroll
    for (int i = 0; i < 12; ++i) {
        const s16x8 af = *reinterpret_cast<const s16x8*>(aimg + (r & 7) * 784 + (i * 4 + q) * 16);
        a = __builtin_amdgcn_mfma_f32_16x16x32_bf16(af, tr_frag(&wimg[(w * 12 + i) * 1024], lane), a, 0, 0, 0);
    }
    if (q < 2) {
#pragma unroll
        for (int j = 0; j < 4; ++j) red[w][0][q * 4 + j][r] = a[j];
    }
    __syncthreads();
    if (w == 0) {
        const int et = lane & 7, ec = (lane >> 3) * 2;
        float o2[2];
#pragma unroll
        for (int e = 0; e < 2; ++e) {
            float y = red[0][0][et][ec + e] + red[1][0][et][ec + e] + red[2][0][et][ec + e] + red[3][0][et][ec + e];
            y = rbf(rbf(y) * sg);
            o2[e] = hv[e] + rbf(lv[e] + y);
        }
        st_sc1_b32(out + (size_t)my_row * D_ + c0 + ec, pack2(o2[0], o2[1]));
    }
}

extern "C" void kernel_launch(void* const* d_in, const int* in_sizes, int n_in,
                              void* d_out, int out_size, void* d_ws, size_t ws_size,
                              hipStream_t stream) {
    const u16* x   = (const u16*)d_in[0];
    const u16* Cre = (const u16*)d_in[1];
    const u16* Cim = (const u16*)d_in[2];
    const u16* Wre = (const u16*)d_in[3];
    const u16* Wim = (const u16*)d_in[4];
    const u16* wr  = (const u16*)d_in[6];
    const u16* wg  = (const u16*)d_in[7];
    const u16* wu  = (const u16*)d_in[8];
    const u16* wd  = (const u16*)d_in[9];
    u16* out = (u16*)d_out;

    char* ws = (char*)d_ws;
    u16* abT = (u16*)ws;
    u16* P = (u16*)(ws + (size_t)2 * D_ * S_ * 2);
    char* tail = ws + (size_t)4 * D_ * S_ * 2;
    u32x2* rg = (u32x2*)tail;
    u32x2* actg = (u32x2*)(tail + 16384);
    unsigned* cnt = (unsigned*)(tail + 16384 + 49152);

    (void)hipFuncSetAttribute((const void*)k_gemm1, hipFuncAttributeMaxDynamicSharedMemorySize, G1_LDS);
    (void)hipFuncSetAttribute((const void*)k_gemm2, hipFuncAttributeMaxDynamicSharedMemorySize, G2_LDS);

    k_gemm1<<<256, 512, G1_LDS, stream>>>(x, Wre, Wim, abT, (u32x4*)tail);
    k_gemm2<<<256, 512, G2_LDS, stream>>>(Cre, Cim, abT, P, wg, wu, wd);
    k_tail<<<224, 256, 0, stream>>>(x, P, wr, wg, wu, wd, out, rg, actg, cnt);
}
```

```cpp
#include <hip/hip_runtime.h>
#include <hip/hip_bf16.h>

typedef unsigned short u16;
typedef __attribute__((ext_vector_type(8))) short s16x8;
typedef __attribute__((ext_vector_type(4))) float f32x4;
typedef __attribute__((ext_vector_type(4))) unsigned int u32x4;
typedef __attribute__((ext_vector_type(2))) unsigned int u32x2;

constexpr int S_ = 2048, D_ = 640, F_ = 1536, C_ = 8;
#define ABP 2048
constexpr int AP_ = ABP;

__device__ __forceinline__ float bf2f(u16 v) { return __uint_as_float(((unsigned)v) << 16); }
__device__ __forceinline__ u16 f2bf(float f) { __bf16 b = (__bf16)f; return __builtin_bit_cast(u16, b); }
__device__ __forceinline__ float rbf(float f) { return bf2f(f2bf(f)); }
__device__ __forceinline__ float lo_f(unsigned w) { return __uint_as_float(w << 16); }
__device__ __forceinline__ float hi_f(unsigned w) { return __uint_as_float(w & 0xffff0000u); }
__device__ __forceinline__ unsigned pack2(float a, float b) { return (unsigned)f2bf(a) | ((unsigned)f2bf(b) << 16); }

typedef __attribute__((ext_vector_type(2))) float f32x2;
typedef __attribute__((ext_vector_type(2))) __bf16 bf16x2_t;
__device__ __forceinline__ unsigned cvt2(f32x2 v) { return __builtin_bit_cast(unsigned, __builtin_convertvector(v, bf16x2_t)); }
__device__ __forceinline__ f32x2 unp2(unsigned w) { f32x2 r; r[0] = lo_f(w); r[1] = hi_f(w); return r; }
__device__ __forceinline__ f32x2 rbf2(f32x2 v) { return unp2(cvt2(v)); }

template <int CTRL> __device__ __forceinline__ unsigned dpp_u(unsigned v) {
    return (unsigned)__builtin_amdgcn_update_dpp(0, (int)v, CTRL, 0xf, 0xf, false);
}
template <int CTRL> __device__ __forceinline__ float dpp_f(float v) { return __uint_as_float(dpp_u<CTRL>(__float_as_uint(v))); }
__device__ __forceinline__ float red16_sum(float v) {
    v += dpp_f<0x128>(v); v += dpp_f<0x124>(v); v += dpp_f<0x122>(v); v += dpp_f<0x121>(v);
    return v;
}

__device__ __forceinline__ void ln_finish(float sum_sq_dev, float mean, float& m_bf, float& rinv) {
    float v = rbf(sum_sq_dev * (1.0f / D_));
    float den = rbf(sqrtf(rbf(v + 1e-5f)));
    m_bf = rbf(mean);
    rinv = 1.0f / den;
}

__device__ __forceinline__ void dma16(const u16* base, unsigned voff, unsigned ldsaddr) {
    unsigned keep;
    asm volatile("s_mov_b32 %0, m0\n\ts_mov_b32 m0, %3\n\ts_nop 0\n\tglobal_load_lds_dwordx4 %1, %2\n\ts_mov_b32 m0, %0"
                 : "=&s"(keep) : "v"(voff), "s"(base), "s"(ldsaddr) : "memory");
}
__device__ __forceinline__ unsigned l2_touch(const u16* base, unsigned voff) {
    unsigned d;
    asm volatile("global_load_dword %0, %1, %2" : "=v"(d) : "v"(voff), "s"(base) : "memory");
    return d;
}
#define l2_touch_done5(a, b, c, d, e) asm volatile("s_waitcnt vmcnt(0)" : "+v"(a), "+v"(b), "+v"(c), "+v"(d), "+v"(e) :: "memory")
#define l2_touch_done1(a) asm volatile("s_waitcnt vmcnt(0)" : "+v"(a) :: "memory")
#define LGKM_BAR() asm volatile("s_waitcnt lgkmcnt(0)\n\ts_barrier" ::: "memory")
#define VM_BAR(N) asm volatile("s_waitcnt vmcnt(" #N ")\n\ts_barrier" ::: "memory")
#define FRAG_BAR(A, B)                                                                              \
    asm volatile("s_waitcnt lgkmcnt(0)\n\ts_barrier"                                                \
                 : "+v"(A[0]), "+v"(A[1]), "+v"(A[2]), "+v"(A[3]), "+v"(B[0]), "+v"(B[1]), "+v"(B[2]), \
                   "+v"(B[3]), "+v"(B[4]), "+v"(B[5]), "+v"(B[6]), "+v"(B[7]), "+v"(B[8]), "+v"(B[9]) \
                 :: "memory")
#define INTERLEAVE_14_20()                                                              \
    do {                                                                                \
        _Pragma("unroll") for (int i_ = 0; i_ < 14; ++i_) {                             \
            __builtin_amdgcn_sched_group_barrier(0x100, 1, 0);                          \
            __builtin_amdgcn_sched_group_barrier(0x008, 1, 0);                          \
        }                                                                               \
        __builtin_amdgcn_sched_group_barrier(0x008, 6, 0);                              \
        __builtin_amdgcn_sched_barrier(0);                                              \
    } while (0)

constexpr int G1_XN = 64 * 1280;
constexpr int G1_SLOT = 160 * 128;
constexpr int G1_LDS = G1_XN + 4 * G1_SLOT;

__global__ __launch_bounds__(512, 2) void k_gemm1(const u16* __restrict__ x, const u16* __restrict__ Wre,
                                                  const u16* __restrict__ Wim, u16* __restrict__ abT,
                                                  u32x4* __restrict__ clr) {
    extern __shared__ __attribute__((aligned(16))) char smem[];
    const int bid = blockIdx.x, xcd = bid & 7, loc = bid >> 3;
    if (bid < 9) {
        const int i = bid * 512 + threadIdx.x;
        if (i < (16384 + 49152 + 4096) / 16) clr[i] = u32x4{0u, 0u, 0u, 0u};
    }
    const int st = (xcd >> 1) * 8 + (loc & 7);
    const int et = (xcd & 1) * 4 + (loc >> 3);
    const int s0 = st * 64, e0 = et * 160;
    const u16* W = (et < 4) ? (Wre + (size_t)e0 * D_) : (Wim + (size_t)(e0 - D_) * D_);
    const int tid = threadIdx.x, lane = tid & 63;
    const int w = __builtin_amdgcn_readfirstlane(tid >> 6);
    const int r = lane & 15, q = lane >> 4;
    const unsigned smem_base = (unsigned)(size_t)(smem);

    const int xrow0 = (w < 4) ? w * 12 : 48 + (w - 4) * 4;
    u32x4 xv[3][5];
    {
        const unsigned xo = (unsigned)(((s0 + xrow0 + q) * D_ + r * 8) * 2);
#pragma unroll
        for (int c = 0; c < 5; ++c)
            asm volatile("global_load_dwordx4 %0, %1, %2" : "=v"(xv[0][c]) : "v"(xo + (unsigned)(c * 256)), "s"(x) : "memory");
        const unsigned xo2 = (w < 4) ? xo + (unsigned)(4 * D_ * 2) : xo;
        const unsigned st2 = (w < 4) ? (unsigned)(4 * D_ * 2) : 0u;
#pragma unroll
        for (int i = 1; i < 3; ++i)
#pragma unroll
            for (int c = 0; c < 5; ++c)
                asm volatile("global_load_dwordx4 %0, %1, %2" : "=v"(xv[i][c]) : "v"(xo2 + (unsigned)(i - 1) * st2 + (unsigned)(c * 256)), "s"(x) : "memory");
    }
    unsigned wv[5];
    const int p = w - 4;
#pragma unroll
    for (int i = 0; i < 5; ++i) {
        const int row = (p & 3) * 40 + i * 8 + (lane >> 3);
        wv[i] = (unsigned)(row * D_ * 2 + (((lane & 7) ^ (row & 7)) * 16));
    }
    const unsigned wl = smem_base + G1_XN + (unsigned)((p & 3) * 5120);
#define G1_ISSUE(K)                                                                     \
    do {                                                                                \
        _Pragma("unroll") for (int i_ = 0; i_ < 5; ++i_)                                \
            dma16(W + (K) * 64, wv[i_], wl + (unsigned)(((K) & 3) * G1_SLOT + i_ * 1024)); \
    } while (0)
    if (w >= 4) { G1_ISSUE(0); G1_ISSUE(1); G1_ISSUE(2); G1_ISSUE(3); }
    unsigned pf = 0;
    if (w < 4) {
        int li = w * 64 + lane;
        li = li < 200 ? li : 199;
        pf = l2_touch(W + (size_t)((loc & 7) * 20) * D_, (unsigned)((li % 20) * D_ * 2 + (li / 20) * 128));
    }

    if (w >= 4) asm volatile("s_waitcnt vmcnt(20)" ::: "memory");
    else asm volatile("s_waitcnt vmcnt(1)" ::: "memory");
    asm volatile("" : "+v"(xv[0][0]), "+v"(xv[0][1]), "+v"(xv[0][2]), "+v"(xv[0][3]), "+v"(xv[0][4]),
                      "+v"(xv[1][0]), "+v"(xv[1][1]), "+v"(xv[1][2]), "+v"(xv[1][3]), "+v"(xv[1][4]),
                      "+v"(xv[2][0]), "+v"(xv[2][1]), "+v"(xv[2][2]), "+v"(xv[2][3]), "+v"(xv[2][4]) :: "memory");
#pragma unroll
    for (int i = 0; i < 3; ++i) {
        if (i > 0 && w >= 4) break;
        f32x2 v[20];
        f32x2 s2 = {0.f, 0.f};
#pragma unroll
        for (int c = 0; c < 5; ++c)
#pragma unroll
            for (int j = 0; j < 4; ++j) { v[c * 4 + j] = unp2(xv[i][c][j]); s2 += v[c * 4 + j]; }
        const float mean = red16_sum(s2[0] + s2[1]) * (1.0f / D_);
        f32x2 q2 = {0.f, 0.f};
#pragma unroll
        for (int j = 0; j < 20; ++j) { const f32x2 d = v[j] - mean; q2 += d * d; }
        float m_bf, rinv;
        ln_finish(red16_sum(q2[0] + q2[1]), mean, m_bf, rinv);
        const int row = xrow0 + i * 4 + q;
#pragma unroll
        for (int c = 0; c < 5; ++c) {
            u32x4 o;
#pragma unroll
            for (int j = 0; j < 4; ++j) o[j] = cvt2(rbf2(v[c * 4 + j] - m_bf) * rinv);
            const int ch = (c * 16 + r) ^ (row & 15);
            *reinterpret_cast<u32x4*>(smem + row * 1280 + ch * 16) = o;
        }
    }

    if (w >= 4) {
        asm volatile("s_waitcnt vmcnt(15) lgkmcnt(0)\n\ts_barrier" ::: "memory");
#pragma unroll
        for (int k = 0; k < 10; ++k) {
            if (k <= 6) VM_BAR(10); else VM_BAR(0);
            if (k + 4 < 10) G1_ISSUE(k + 4);
        }
        return;
    }
    l2_touch_done1(pf);
    const int ws = w & 1, we = w >> 1;
    f32x4 acc[2][5];
#pragma unroll
    for (int m = 0; m < 2; ++m)
#pragma unroll
        for (int n = 0; n < 5; ++n) acc[m][n] = f32x4{0.f, 0.f, 0.f, 0.f};
    const char* xbase = smem + (ws * 32 + r) * 1280;
    const char* wbase = smem + G1_XN + (we * 80 + r) * 128;
    s16x8 xf[2][4], wf[2][10];
#define G1_READ(B, K)                                                                   \
    do {                                                                                \
        _Pragma("unroll") for (int h_ = 0; h_ < 2; ++h_) {                              \
            _Pragma("unroll") for (int m_ = 0; m_ < 2; ++m_)                            \
                xf[B][m_ * 2 + h_] = *reinterpret_cast<const s16x8*>(xbase + m_ * 16 * 1280 + ((((K) * 8 + h_ * 4 + q) ^ r) * 16)); \
            _Pragma("unroll") for (int n_ = 0; n_ < 5; ++n_)                            \
                wf[B][n_ * 2 + h_] = *reinterpret_cast<const s16x8*>(wbase + ((K) & 3) * G1_SLOT + n_ * 2048 + (((h_ * 4 + q) ^ (r & 7)) * 16)); \
        }                                                                               \
    } while (0)
    LGKM_BAR();
    G1_READ(0, 0);
#pragma unroll
    for (int k = 0; k < 10; ++k) {
        FRAG_BAR(xf[k & 1], wf[k & 1]);
        if (k + 1 < 10) G1_READ((k + 1) & 1, k + 1);
#pragma unroll
        for (int h = 0; h < 2; ++h)
#pragma unroll
            for (int m = 0; m < 2; ++m)
#pragma unroll
                for (int n = 0; n < 5; ++n)
                    acc[m][n] = __builtin_amdgcn_mfma_f32_16x16x32_bf16(xf[k & 1][m * 2 + h], wf[k & 1][n * 2 + h], acc[m][n], 0, 0, 0);
        if (k + 1 < 10) INTERLEAVE_14_20();
    }
    char* img = smem + G1_XN + 2 * G1_SLOT + w * 5120;
#pragma unroll
    for (int m = 0; m < 2; ++m)
#pragma unroll
        for (int n = 0; n < 5; ++n) {
            u32x2 o;
            o[0] = pack2(acc[m][n][0], acc[m][n][1]);
            o[1] = pack2(acc[m][n][2], acc[m][n][3]);
            *reinterpret_cast<u32x2*>(img + (n * 16 + r) * 64 + m * 32 + q * 8) = o;
        }
    u16* dst = abT + (size_t)(e0 + we * 80) * AP_ + s0 + ws * 32;
#pragma unroll
    for (int i = 0; i < 5; ++i) {
        const int c = i * 64 + lane;
        const u32x4 v = *reinterpret_cast<const u32x4*>(img + c * 16);
        __builtin_nontemporal_store(v, reinterpret_cast<u32x4*>(dst + (size_t)(c >> 2) * AP_ + (c & 3) * 8));
    }
}

#define G2_ROT 0
#define G2_T2 8
#define G2_T3 16
constexpr int G2_SLOT = (128 + 80) * 128;
constexpr int G2_NST = 6;
constexpr int G2_LDS = G2_NST * G2_SLOT;

__global__ __launch_bounds__(512, 2) void k_gemm2(const u16* __restrict__ Cre, const u16* __restrict__ Cim,
                                                  const u16* __restrict__ abT, u16* __restrict__ P,
                                                  const u16* __restrict__ wg, const u16* __restrict__ wu,
                                                  const u16* __restrict__ wd) {
    extern __shared__ __attribute__((aligned(16))) char smem[];
    const int bid = blockIdx.x, xcd = bid & 7, loc = bid >> 3;
    const int part = xcd & 1;
    const int tp = (xcd >> 1) * 4 + (loc >> 3);
    const int ct = loc & 7;
    const int t0 = tp * 128, e0 = ct * 80;
    const int tid = threadIdx.x, lane = tid & 63;
    const int w = __builtin_amdgcn_readfirstlane(tid >> 6);
    const int r = lane & 15, q = lane >> 4;
    const unsigned smem_base = (unsigned)(size_t)(smem);

    if (w >= 4) {
        __builtin_amdgcn_s_setprio(2);
        const int p = w - 4;
        const u16* Cb = (part ? Cim : Cre) + (size_t)t0 * S_;
        const u16* Bb = abT + (size_t)(part * D_ + e0) * AP_;
        unsigned av[4], bv[3];
#pragma unroll
        for (int i = 0; i < 4; ++i) {
            const int row = p * 32 + i * 8 + (lane >> 3);
            av[i] = (unsigned)(row * S_ * 2 + (((lane & 7) ^ (row & 7)) * 16));
        }
#pragma unroll
        for (int i = 0; i < 3; ++i) {
            const int o = p * 2560 + i * 1024 + lane * 16;
            const int row = o >> 7, cs = (o >> 4) & 7;
            bv[i] = (unsigned)(row * AP_ * 2 + ((cs ^ (row & 7)) * 16));
        }
        const int rot = G2_ROT;
        const unsigned la = smem_base + (unsigned)(p * 4096);
        const unsigned lb = smem_base + 16384u + (unsigned)(p * 2560);
#define G2_ISSUE(K, SO)                                                                 \
        do {                                                                            \
            const int kk_ = ((K) + rot) & 31;                                           \
            const u16* cb_ = Cb + kk_ * 64;                                             \
            const u16* bb_ = Bb + kk_ * 64;                                             \
            _Pragma("unroll") for (int i_ = 0; i_ < 4; ++i_) dma16(cb_, av[i_], la + (SO) + i_ * 1024u); \
            dma16(bb_, bv[0], lb + (SO));                                               \
            dma16(bb_, bv[1], lb + (SO) + 1024u);                                       \
            if (lane < 32) dma16(bb_, bv[2], lb + (SO) + 2048u);                        \
        } while (0)
#pragma unroll
        for (int k = 0; k < G2_NST; ++k) G2_ISSUE(k, (unsigned)(k * G2_SLOT));
        VM_BAR(35);
        unsigned so = 0;
        for (int k = 0; k < 32; ++k) {
            if (k <= 26) VM_BAR(28); else VM_BAR(0);
            if (k + G2_NST < 32) G2_ISSUE(k + G2_NST, so);
            so = (so == (unsigned)((G2_NST - 1) * G2_SLOT)) ? 0u : so + (unsigned)G2_SLOT;
        }
        return;
    }
    const u16 *pfp0, *pfp1;
    {
        const int l0 = w * 9 + (lane >> 3), l1 = w * 9 + 8;
        const u16* Cp = (part ? Cim : Cre) + (size_t)(t0 + ct * 16) * S_ + (lane & 7) * 64;
        const u16* Bp = abT + (size_t)(part * D_ + e0 + (loc >> 3) * 20) * AP_ + (lane & 7) * 64;
        pfp0 = (l0 < 16) ? (Cp + (size_t)l0 * S_) : (Bp + (size_t)(l0 - 16) * AP_);
        pfp1 = (l1 < 16) ? (Cp + (size_t)l1 * S_) : (Bp + (size_t)(l1 - 16) * AP_);
    }
    unsigned pfd = 0;
#define G2_TOUCH(J)                                                                                     \
    do {                                                                                                \
        asm volatile("global_load_dword %0, %1, off" : "+v"(pfd) : "v"(pfp0 + (J) * 512) : "memory");   \
        asm volatile("global_load_dword %0, %1, off" : "+v"(pfd) : "v"(pfp1 + (J) * 512) : "memory");   \
    } while (0)
    G2_TOUCH(0); G2_TOUCH(1);
    f32x4 acc[5][2];
#pragma unroll
    for (int n = 0; n < 5; ++n)
#pragma unroll
        for (int m = 0; m < 2; ++m) acc[n][m] = f32x4{0.f, 0.f, 0.f, 0.f};
    const int rd0 = r * 128 + ((q ^ (r & 7)) * 16);
    const int rd1 = r * 128 + (((q + 4) ^ (r & 7)) * 16);
    const int aw = w * 4096;
    s16x8 cf[2][4], ef[2][10];
#define G2_READ(B, SO)                                                                  \
    do {                                                                                \
        const char* s0_ = smem + (SO) + rd0;                                            \
        const char* s1_ = smem + (SO) + rd1;                                            \
        _Pragma("unroll") for (int m_ = 0; m_ < 2; ++m_) {                              \
            cf[B][m_ * 2 + 0] = *reinterpret_cast<const s16x8*>(s0_ + aw + m_ * 2048);  \
            cf[B][m_ * 2 + 1] = *reinterpret_cast<const s16x8*>(s1_ + aw + m_ * 2048);  \
        }                                                                               \
        _Pragma("unroll") for (int n_ = 0; n_ < 5; ++n_) {                              \
            ef[B][n_ * 2 + 0] = *reinterpret_cast<const s16x8*>(s0_ + 16384 + n_ * 2048); \
            ef[B][n_ * 2 + 1] = *reinterpret_cast<const s16x8*>(s1_ + 16384 + n_ * 2048); \
        }                                                                               \
    } while (0)
#define G2_MMA(B)                                                                       \
    do {                                                                                \
        _Pragma("unroll") for (int h_ = 0; h_ < 2; ++h_)                                \
        _Pragma("unroll") for (int n_ = 0; n_ < 5; ++n_)                                \
        _Pragma("unroll") for (int m_ = 0; m_ < 2; ++m_)                                \
            acc[n_][m_] = __builtin_amdgcn_mfma_f32_16x16x32_bf16(ef[B][n_ * 2 + h_], cf[B][m_ * 2 + h_], acc[n_][m_], 0, 0, 0); \
    } while (0)
    LGKM_BAR();
    G2_READ(0, 0);
    int so = G2_SLOT;
    for (int k = 0; k < 32; k += 2) {
        FRAG_BAR(cf[0], ef[0]);
        if (k == G2_T2) G2_TOUCH(2);
        if (k == G2_T3) G2_TOUCH(3);
        if (k == 26) {
            const int li = (bid * 4 + w) * 64 + lane;
            const int mi = li / 15360;
            const u16* wp = (mi == 0 ? wg : (mi == 1 ? wu : wd)) + (size_t)(mi < 3 ? li - mi * 15360 : lane) * 64;
            asm volatile("global_load_dword %0, %1, off" : "+v"(pfd) : "v"(wp) : "memory");
        }
        G2_READ(1, so);
        so = (so == (G2_NST - 1) * G2_SLOT) ? 0 : so + G2_SLOT;
        G2_MMA(0);
        INTERLEAVE_14_20();
        FRAG_BAR(cf[1], ef[1]);
        G2_READ(0, so);
        so = (so == (G2_NST - 1) * G2_SLOT) ? 0 : so + G2_SLOT;
        G2_MMA(1);
        INTERLEAVE_14_20();
    }
    l2_touch_done1(pfd);
    char* img = smem + 3 * G2_SLOT + w * 5120;
#pragma unroll
    for (int m = 0; m < 2; ++m)
#pragma unroll
        for (int n = 0; n < 5; ++n) {
            u32x2 o;
            o[0] = pack2(acc[n][m][0], acc[n][m][1]);
            o[1] = pack2(acc[n][m][2], acc[n][m][3]);
            *reinterpret_cast<u32x2*>(img + (m * 16 + r) * 160 + n * 32 + q * 8) = o;
        }
    u16* dst = P + (size_t)part * S_ * D_ + (size_t)(t0 + w * 32) * D_ + e0;
#pragma unroll
    for (int i = 0; i < 5; ++i) {
        const int c = i * 64 + lane;
        const u32x4 v = *reinterpret_cast<const u32x4*>(img + c * 16);
        const int row = c / 10;
        __builtin_nontemporal_store(v, reinterpret_cast<u32x4*>(dst + (size_t)row * D_ + (c - row * 10) * 8));
    }
}

__device__ __forceinline__ unsigned topk_key(float v, int idx) {
    unsigned b = __float_as_uint(v) >> 16;
    b = (b & 0x8000u) ? (~b & 0xffffu) : (b | 0x8000u);
    return (b << 11) | (unsigned)(S_ - 1 - idx);
}
__device__ __forceinline__ unsigned umax_(unsigned a, unsigned b) { return a > b ? a : b; }
__device__ __forceinline__ unsigned wave_max_u32(unsigned v) {
    v = umax_(v, dpp_u<0x128>(v)); v = umax_(v, dpp_u<0x124>(v)); v = umax_(v, dpp_u<0x122>(v)); v = umax_(v, dpp_u<0x121>(v));
    v = umax_(v, (unsigned)__builtin_amdgcn_update_dpp(0, (int)v, 0x142, 0xa, 0xf, false));
    v = umax_(v, (unsigned)__builtin_amdgcn_update_dpp(0, (int)v, 0x143, 0xc, 0xf, false));
    return __builtin_amdgcn_readlane(v, 63);
}
typedef __attribute__((ext_vector_type(4))) short s16x4;
typedef __attribute__((address_space(3))) s16x4* lds_s16x4_p;
__device__ __forceinline__ s16x8 tr_frag(const char* img, int lane) {
    const int i = lane & 15, g = lane >> 4;
    const char* a = img + (g * 8 + (i >> 2)) * 32 + (i & 3) * 8;
    const s16x4 lo = __builtin_amdgcn_ds_read_tr16_b64_v4i16((lds_s16x4_p)(a));
    const s16x4 hi = __builtin_amdgcn_ds_read_tr16_b64_v4i16((lds_s16x4_p)(a + 128));
    return s16x8{lo[0], lo[1], lo[2], lo[3], hi[0], hi[1], hi[2], hi[3]};
}

constexpr int CNT_STRIDE = 64;
constexpr unsigned RG_TAG = 0x5AA5u;
constexpr unsigned ACT_TAG = 0x7C3E91B5u;
constexpr unsigned SPIN_CAP = 1u << 20;
__device__ __forceinline__ void st_sc1_b128(void* p, u32x4 v) {
    asm volatile("global_store_dwordx4 %0, %1, off sc1\n\ts_nop 1" :: "v"(p), "v"(v) : "memory");
}
__device__ __forceinline__ void st_sc1_b64(void* p, u32x2 v) {
    asm volatile("global_store_dwordx2 %0, %1, off sc1\n\ts_nop 1" :: "v"(p), "v"(v) : "memory");
}
__device__ __forceinline__ void st_sc1_b32(void* p, unsigned v) {
    asm volatile("global_store_dword %0, %1, off sc1\n\ts_nop 1" :: "v"(p), "v"(v) : "memory");
}

__device__ __forceinline__ int route_top8(const u32x2* rg, unsigned* cand, u32x2* lrg, int w, int lane, u32x2& pay) {
    u32x4 g[4];
    for (unsigned spins = 0; spins < SPIN_CAP; ++spins) {
#pragma unroll
        for (int i = 0; i < 4; ++i)
            asm volatile("global_load_dwordx4 %0, %1, %2 sc1" : "=v"(g[i]) : "v"((unsigned)((w * 256 + i * 64 + lane) * 16)), "s"(rg) : "memory");
        asm volatile("s_waitcnt vmcnt(0)" : "+v"(g[0]), "+v"(g[1]), "+v"(g[2]), "+v"(g[3]) :: "memory");
        bool ok = true;
#pragma unroll
        for (int i = 0; i < 4; ++i) ok = ok && ((g[i][1] >> 16) == RG_TAG) && ((g[i][3] >> 16) == RG_TAG);
        if (__all(ok)) break;
        __builtin_amdgcn_s_sleep(2);
    }
    unsigned key[8];
#pragma unroll
    for (int i = 0; i < 4; ++i) {
        const int idx = (w * 256 + i * 64 + lane) * 2;
        key[2 * i] = topk_key(lo_f(g[i][0]), idx);
        key[2 * i + 1] = topk_key(lo_f(g[i][2]), idx + 1);
        *reinterpret_cast<u32x4*>(lrg + idx) = g[i];
    }
    unsigned prev = 1u << 27, my_key = 0;
#pragma unroll
    for (int t = 0; t < 8; ++t) {
        unsigned m = 0;
#pragma unroll
        for (int i = 0; i < 8; ++i) m = umax_(m, key[i] - prev);
        prev += wave_max_u32(m);
        my_key = (lane == t) ? prev : my_key;
    }
    if (lane < 8) cand[w * 8 + lane] = my_key;
    __syncthreads();
    const unsigned ck = cand[lane & 31];
    prev = 1u << 27; my_key = 0;
#pragma unroll
    for (int t = 0; t < 8; ++t) {
        prev += wave_max_u32(ck - prev);
        my_key = ((lane & 7) == t) ? prev : my_key;
    }
    const int row = (S_ - 1 - (int)(my_key & 2047u)) & 2047;
    pay = lrg[row];
    return row;
}

__global__ __launch_bounds__(256) void k_tail(const u16* __restrict__ x, const u16* __restrict__ P,
                                              const u16* __restrict__ wr, const u16* __restrict__ wg,
                                              const u16* __restrict__ wu, const u16* __restrict__ wd,
                                              u16* out, u32x2* rg, u32x2* actg, unsigned* cnt) {
    __shared__ __attribute__((aligned(16))) char wimg[48 * 1024];
    __shared__ __attribute__((aligned(16))) u32x2 lrg[2048];
    __shared__ __attribute__((aligned(16))) char tok[4 * 8 * 784];
    __shared__ float red[4][2][8][16];
    __shared__ unsigned cand[32];
    __shared__ unsigned arrived;
    const int bid = blockIdx.x;
    const int tid = threadIdx.x, lane = tid & 63, r = lane & 15, q = lane >> 4;
    const int w = __builtin_amdgcn_readfirstlane(tid >> 6);
    const unsigned l0 = (unsigned)(size_t)(&wimg[0]);

    const bool up_role = bid >= 128;
    if (!up_role) {
        if (tid == 0) arrived = 0u;
        __syncthreads();
        {
            const int row = bid * 16 + w * 4 + q;
            const size_t ro = (size_t)row * D_;
            u32x4 xv[5], p1[5], p2[5], wv[5];
    #pragma unroll
            for (int c = 0; c < 5; ++c) {
                const int o = (c * 16 + r) * 8;
                xv[c] = *reinterpret_cast<const u32x4*>(x + ro + o);
                p1[c] = *reinterpret_cast<const u32x4*>(P + ro + o);
                p2[c] = *reinterpret_cast<const u32x4*>(P + (size_t)S_ * D_ + ro + o);
                wv[c] = *reinterpret_cast<const u32x4*>(wr + o);
            }
            f32x2 h[20];
            f32x2 s2 = {0.f, 0.f};
    #pragma unroll
            for (int c = 0; c < 5; ++c)
    #pragma unroll
                for (int j = 0; j < 4; ++j) {
                    h[c * 4 + j] = rbf2(unp2(xv[c][j]) + rbf2(unp2(p1[c][j]) - unp2(p2[c][j])));
                    s2 += h[c * 4 + j];
                }
            const float mean = red16_sum(s2[0] + s2[1]) * (1.0f / D_);
            f32x2 q2 = {0.f, 0.f};
    #pragma unroll
            for (int i = 0; i < 20; ++i) { const f32x2 d = h[i] - mean; q2 += d * d; }
            const float sq = red16_sum(q2[0] + q2[1]);
            const float m_bf = rbf(mean);
            const float den = rbf(sqrtf(rbf(rbf(sq * (1.0f / D_)) + 1e-5f)));
            const float rinv = 1.0f / den;
            f32x2 d2 = {0.f, 0.f};
            u32x4 o[5];
    #pragma unroll
            for (int c = 0; c < 5; ++c)
    #pragma unroll
                for (int j = 0; j < 4; ++j) {
                    const f32x2 a = rbf2(rbf2(h[c * 4 + j] - m_bf) * rinv);
                    d2 += a * unp2(wv[c][j]);
                    o[c][j] = cvt2(h[c * 4 + j] + a);
                }
            const float dot = red16_sum(d2[0] + d2[1]);
            if (r == 0) {
                u32x2 gv; gv[0] = pack2(dot, m_bf); gv[1] = (unsigned)f2bf(den) | (RG_TAG << 16);
                st_sc1_b64(rg + row, gv);
            }
    #pragma unroll
            for (int c = 0; c < 5; ++c) st_sc1_b128(out + ro + (c * 16 + r) * 8, o[c]);
            asm volatile("s_waitcnt vmcnt(0)" ::: "memory");
            if (lane == 0) {
                const unsigned old = __hip_atomic_fetch_add(&arrived, 1u, __ATOMIC_RELAXED, __HIP_MEMORY_SCOPE_WORKGROUP);
                if (old == 3u) __hip_atomic_fetch_add(cnt + (bid & 7) * CNT_STRIDE, 1u, __ATOMIC_RELAXED, __HIP_MEMORY_SCOPE_AGENT);
            }
        }
    if (bid >= 40) return;
    }
    const int ju = bid - 128;
    const int c0 = up_role ? ((ju & 7) * 12 + (ju >> 3)) * 16 : ((bid & 7) * 5 + (bid >> 3)) * 16;
    if (up_role) {
        const unsigned voff = (unsigned)(((w * 160 + (lane >> 1)) * F_ + c0 + (lane & 1) * 8) * 2);
#pragma unroll
        for (int i = 0; i < 5; ++i) {
            dma16(wg, voff + (unsigned)(i * 32 * F_ * 2), l0 + (unsigned)((w * 5 + i) * 1024));
            dma16(wu, voff + (unsigned)(i * 32 * F_ * 2), l0 + (unsigned)(20480 + (w * 5 + i) * 1024));
        }
    } else {
        const unsigned voff = (unsigned)(((w * 384 + (lane >> 1)) * D_ + c0 + (lane & 1) * 8) * 2);
#pragma unroll
        for (int i = 0; i < 12; ++i) dma16(wd, voff + (unsigned)(i * 32 * D_ * 2), l0 + (unsigned)((w * 12 + i) * 1024));
    }
    u32x2 pay;
    const int my_row = route_top8(rg, cand, lrg, w, lane, pay);

    if (up_role) {
        {
            u32x4 xv[3], p1[3], p2[3];
            int tk[3], ch[3];
#pragma unroll
            for (int i = 0; i < 3; ++i) {
                int c = i * 64 + lane; c = c < 160 ? c : 159;
                tk[i] = 2 * w + (c >= 80 ? 1 : 0); ch[i] = c >= 80 ? c - 80 : c;
                const int rw = __shfl(my_row, tk[i]);
                const size_t o = (size_t)rw * D_ + ch[i] * 8;
                xv[i] = *reinterpret_cast<const u32x4*>(x + o);
                p1[i] = *reinterpret_cast<const u32x4*>(P + o);
                p2[i] = *reinterpret_cast<const u32x4*>(P + (size_t)S_ * D_ + o);
            }
#pragma unroll
            for (int i = 0; i < 3; ++i) {
                const float sm = hi_f(__shfl(pay[0], tk[i])), sr = 1.0f / lo_f(__shfl(pay[1], tk[i]));
                u32x4 lp;
#pragma unroll
                for (int j = 0; j < 4; ++j) {
                    const f32x2 hh = rbf2(unp2(xv[i][j]) + rbf2(unp2(p1[i][j]) - unp2(p2[i][j])));
                    lp[j] = cvt2(rbf2(hh - sm) * sr);
                }
                *reinterpret_cast<u32x4*>(tok + tk[i] * 1296 + ch[i] * 16) = lp;
            }
        }
        __syncthreads();
        f32x4 ag = {0.f, 0.f, 0.f, 0.f}, au = {0.f, 0.f, 0.f, 0.f};
#pragma unroll
        for (int i = 0; i < 5; ++i) {
            const s16x8 af = *reinterpret_cast<const s16x8*>(tok + (r & 7) * 1296 + ((w * 5 + i) * 4 + q) * 16);
            ag = __builtin_amdgcn_mfma_f32_16x16x32_bf16(af, tr_frag(&wimg[(w * 5 + i) * 1024], lane), ag, 0, 0, 0);
            au = __builtin_amdgcn_mfma_f32_16x16x32_bf16(af, tr_frag(&wimg[20480 + (w * 5 + i) * 1024], lane), au, 0, 0, 0);
        }
        if (q < 2) {
#pragma unroll
            for (int j = 0; j < 4; ++j) { red[w][0][q * 4 + j][r] = ag[j]; red[w][1][q * 4 + j][r] = au[j]; }
        }
        __syncthreads();
        if (tid < 64) {
            const int t = tid >> 3, c = (tid & 7) * 2;
            float o2[2];
#pragma unroll
            for (int e = 0; e < 2; ++e) {
                float g = red[0][0][t][c + e] + red[1][0][t][c + e] + red[2][0][t][c + e] + red[3][0][t][c + e];
                float u = red[0][1][t][c + e] + red[1][1][t][c + e] + red[2][1][t][c + e] + red[3][1][t][c + e];
                g = rbf(g); u = rbf(u);
                o2[e] = rbf(g / (1.0f + __expf(-g))) * u;
            }
            u32x2 gv; gv[0] = pack2(o2[0], o2[1]); gv[1] = ACT_TAG;
            st_sc1_b64(actg + t * (F_ / 2) + (c0 + c) / 2, gv);
        }
        return;
    }

    float hv[2], lv[2], sg;
    {
        const size_t o = (size_t)my_row * D_ + c0 + (lane >> 3) * 2;
        const unsigned xv = *reinterpret_cast<const unsigned*>(x + o);
        const unsigned p1 = *reinterpret_cast<const unsigned*>(P + o);
        const unsigned p2 = *reinterpret_cast<const unsigned*>(P + (size_t)S_ * D_ + o);
        const float sm = hi_f(pay[0]), sr = 1.0f / lo_f(pay[1]);
        hv[0] = rbf(lo_f(xv) + rbf(lo_f(p1) - lo_f(p2)));
        hv[1] = rbf(hi_f(xv) + rbf(hi_f(p1) - hi_f(p2)));
        lv[0] = rbf(rbf(hv[0] - sm) * sr); lv[1] = rbf(rbf(hv[1] - sm) * sr);
        sg = rbf(1.0f / (1.0f + __expf(-lo_f(pay[0]))));
    }
    if (w == 0) {
        for (unsigned spins = 0; spins < SPIN_CAP; ++spins) {
            unsigned v = 0;
            if (lane < 8) v = __hip_atomic_load(cnt + lane * CNT_STRIDE, __ATOMIC_RELAXED, __HIP_MEMORY_SCOPE_AGENT);
            v += dpp_u<0x0B1>(v); v += dpp_u<0x04E>(v); v += dpp_u<0x141>(v);
            if (__builtin_amdgcn_readfirstlane(v) >= 128u) break;
            __builtin_amdgcn_s_sleep(2);
        }
    }
    for (unsigned spins = 0; spins < SPIN_CAP; ++spins) {
        u32x2 g1;
        asm volatile("global_load_dwordx2 %0, %1, off sc1\n\ts_waitcnt vmcnt(0)" : "=v"(g1)
                     : "v"(actg + 7 * (F_ / 2) + w * 192 + (lane < 24 ? lane : 23) * 8 + 7) : "memory");
        if (__all(g1[1] == ACT_TAG)) break;
        __builtin_amdgcn_s_sleep(1);
    }
    u32x4 ga[12];
    for (unsigned spins = 0; spins < SPIN_CAP; ++spins) {
#pragma unroll
        for (int i = 0; i < 12; ++i) {
            const int c = i * 64 + lane, t = c / 96;
            asm volatile("global_load_dwordx4 %0, %1, off sc1" : "=v"(ga[i]) : "v"(actg + t * (F_ / 2) + w * 192 + (c - t * 96) * 2) : "memory");
        }
        asm volatile("s_waitcnt vmcnt(0)"
                     : "+v"(ga[0]), "+v"(ga[1]), "+v"(ga[2]), "+v"(ga[3]), "+v"(ga[4]), "+v"(ga[5]),
                       "+v"(ga[6]), "+v"(ga[7]), "+v"(ga[8]), "+v"(ga[9]), "+v"(ga[10]), "+v"(ga[11]) :: "memory");
        bool ok = true;
#pragma unroll
        for (int i = 0; i < 12; ++i) ok = ok && (ga[i][1] == ACT_TAG) && (ga[i][3] == ACT_TAG);
        if (__all(ok)) break;
        __builtin_amdgcn_s_sleep(1);
    }
    char* aimg = tok + w * (8 * 784);
#pragma unroll
    for (int i = 0; i < 12; ++i) {
        const int c = i * 64 + lane, t = c / 96;
        u32x2 v; v[0] = ga[i][0]; v[1] = ga[i][2];
        *reinterpret_cast<u32x2*>(aimg + t * 784 + (c - t * 96) * 8) = v;
    }
    f32x4 a = {0.f, 0.f, 0.f, 0.f};
#pragma unroll
    for (int i = 0; i < 12; ++i) {
        const s16x8 af = *reinterpret_cast<const s16x8*>(aimg + (r & 7) * 784 + (i * 4 + q) * 16);
        a = __builtin_amdgcn_mfma_f32_16x16x32_bf16(af, tr_frag(&wimg[(w * 12 + i) * 1024], lane), a, 0, 0, 0);
    }
    if (q < 2) {
#pragma unroll
        for (int j = 0; j < 4; ++j) red[w][0][q * 4 + j][r] = a[j];
    }
    __syncthreads();
    if (w == 0) {
        const int et = lane & 7, ec = (lane >> 3) * 2;
        float o2[2];
#pragma unroll
        for (int e = 0; e < 2; ++e) {
            float y = red[0][0][et][ec + e] + red[1][0][et][ec + e] + red[2][0][et][ec + e] + red[3][0][et][ec + e];
            y = rbf(rbf(y) * sg);
            o2[e] = hv[e] + rbf(lv[e] + y);
        }
        st_sc1_b32(out + (size_t)my_row * D_ + c0 + ec, pack2(o2[0], o2[1]));
    }
}

extern "C" void kernel_launch(void* const* d_in, const int* in_sizes, int n_in,
                              void* d_out, int out_size, void* d_ws, size_t ws_size,
                              hipStream_t stream) {
    const u16* x   = (const u16*)d_in[0];
    const u16* Cre = (const u16*)d_in[1];
    const u16* Cim = (const u16*)d_in[2];
    const u16* Wre = (const u16*)d_in[3];
    const u16* Wim = (const u16*)d_in[4];
    const u16* wr  = (const u16*)d_in[6];
    const u16* wg  = (const u16*)d_in[7];
    const u16* wu  = (const u16*)d_in[8];
    const u16* wd  = (const u16*)d_in[9];
    u16* out = (u16*)d_out;

    char* ws = (char*)d_ws;
    u16* abT = (u16*)ws;
    u16* P = (u16*)(ws + (size_t)2 * D_ * AP_ * 2);
    char* tail = ws + (size_t)2 * D_ * AP_ * 2 + (size_t)2 * D_ * S_ * 2;
    u32x2* rg = (u32x2*)tail;
    u32x2* actg = (u32x2*)(tail + 16384);
    unsigned* cnt = (unsigned*)(tail + 16384 + 49152);

    (void)hipFuncSetAttribute((const void*)k_gemm1, hipFuncAttributeMaxDynamicSharedMemorySize, G1_LDS);
    (void)hipFuncSetAttribute((const void*)k_gemm2, hipFuncAttributeMaxDynamicSharedMemorySize, G2_LDS);

    k_gemm1<<<256, 512, G1_LDS, stream>>>(x, Wre, Wim, abT, (u32x4*)tail);
    k_gemm2<<<256, 512, G2_LDS, stream>>>(Cre, Cim, abT, P, wg, wu, wd);
    k_tail<<<224, 256, 0, stream>>>(x, P, wr, wg, wu, wd, out, rg, actg, cnt);
}
```

```cpp
#include <hip/hip_runtime.h>
#include <hip/hip_bf16.h>

typedef unsigned short u16;
typedef __attribute__((ext_vector_type(8))) short s16x8;
typedef __attribute__((ext_vector_type(4))) float f32x4;
typedef __attribute__((ext_vector_type(4))) unsigned int u32x4;
typedef __attribute__((ext_vector_type(2))) unsigned int u32x2;

constexpr int S_ = 2048, D_ = 640, F_ = 1536, C_ = 8;
constexpr int AP_ = 2048;

__device__ __forceinline__ float bf2f(u16 v) { return __uint_as_float(((unsigned)v) << 16); }
__device__ __forceinline__ u16 f2bf(float f) { __bf16 b = (__bf16)f; return __builtin_bit_cast(u16, b); }
__device__ __forceinline__ float rbf(float f) { return bf2f(f2bf(f)); }
__device__ __forceinline__ float lo_f(unsigned w) { return __uint_as_float(w << 16); }
__device__ __forceinline__ float hi_f(unsigned w) { return __uint_as_float(w & 0xffff0000u); }
__device__ __forceinline__ unsigned pack2(float a, float b) { return (unsigned)f2bf(a) | ((unsigned)f2bf(b) << 16); }

typedef __attribute__((ext_vector_type(2))) float f32x2;
typedef __attribute__((ext_vector_type(2))) __bf16 bf16x2_t;
__device__ __forceinline__ unsigned cvt2(f32x2 v) { return __builtin_bit_cast(unsigned, __builtin_convertvector(v, bf16x2_t)); }
__device__ __forceinline__ f32x2 unp2(unsigned w) { f32x2 r; r[0] = lo_f(w); r[1] = hi_f(w); return r; }
__device__ __forceinline__ f32x2 rbf2(f32x2 v) { return unp2(cvt2(v)); }

template <int CTRL> __device__ __forceinline__ unsigned dpp_u(unsigned v) {
    return (unsigned)__builtin_amdgcn_update_dpp(0, (int)v, CTRL, 0xf, 0xf, false);
}
template <int CTRL> __device__ __forceinline__ float dpp_f(float v) { return __uint_as_float(dpp_u<CTRL>(__float_as_uint(v))); }
__device__ __forceinline__ float red16_sum(float v) {
    v += dpp_f<0x128>(v); v += dpp_f<0x124>(v); v += dpp_f<0x122>(v); v += dpp_f<0x121>(v);
    return v;
}

__device__ __forceinline__ void ln_finish(float sum_sq_dev, float mean, float& m_bf, float& rinv) {
    float v = rbf(sum_sq_dev * (1.0f / D_));
    float den = rbf(sqrtf(rbf(v + 1e-5f)));
    m_bf = rbf(mean);
    rinv = 1.0f / den;
}

__device__ __forceinline__ void dma16(const u16* base, unsigned voff, unsigned ldsaddr) {
    unsigned keep;
    asm volatile("s_mov_b32 %0, m0\n\ts_mov_b32 m0, %3\n\ts_nop 0\n\tglobal_load_lds_dwordx4 %1, %2\n\ts_mov_b32 m0, %0"
                 : "=&s"(keep) : "v"(voff), "s"(base), "s"(ldsaddr) : "memory");
}
__device__ __forceinline__ unsigned l2_touch(const u16* base, unsigned voff) {
    unsigned d;
    asm volatile("global_load_dword %0, %1, %2" : "=v"(d) : "v"(voff), "s"(base) : "memory");
    return d;
}
#define l2_touch_done1(a) asm volatile("s_waitcnt vmcnt(0)" : "+v"(a) :: "memory")
#define LGKM_BAR() asm volatile("s_waitcnt lgkmcnt(0)\n\ts_barrier" ::: "memory")
#define VM_BAR(N) asm volatile("s_waitcnt vmcnt(" #N ")\n\ts_barrier" ::: "memory")
#define FRAG_BAR(A, B)                                                                              \
    asm volatile("s_waitcnt lgkmcnt(0)\n\ts_barrier"                                                \
                 : "+v"(A[0]), "+v"(A[1]), "+v"(A[2]), "+v"(A[3]), "+v"(B[0]), "+v"(B[1]), "+v"(B[2]), \
                   "+v"(B[3]), "+v"(B[4]), "+v"(B[5]), "+v"(B[6]), "+v"(B[7]), "+v"(B[8]), "+v"(B[9]) \
                 :: "memory")
#define INTERLEAVE_14_20()                                                              \
    do {                                                                                \
        _Pragma("unroll") for (int i_ = 0; i_ < 14; ++i_) {                             \
            __builtin_amdgcn_sched_group_barrier(0x100, 1, 0);                          \
            __builtin_amdgcn_sched_group_barrier(0x008, 1, 0);                          \
        }                                                                               \
        __builtin_amdgcn_sched_group_barrier(0x008, 6, 0);                              \
        __builtin_amdgcn_sched_barrier(0);                                              \
    } while (0)

constexpr int G1_XN = 64 * 1280;
constexpr int G1_SLOT = 160 * 128;
constexpr int G1_LDS = G1_XN + 4 * G1_SLOT;

__global__ __launch_bounds__(512, 2) void k_gemm1(const u16* __restrict__ x, const u16* __restrict__ Wre,
                                                  const u16* __restrict__ Wim, u16* __restrict__ abT,
                                                  u32x4* __restrict__ clr) {
    extern __shared__ __attribute__((aligned(16))) char smem[];
    const int bid = blockIdx.x, xcd = bid & 7, loc = bid >> 3;
    if (bid < 9) {
        const int i = bid * 512 + threadIdx.x;
        if (i < (16384 + 49152 + 4096) / 16) clr[i] = u32x4{0u, 0u, 0u, 0u};
    }
    const int st = (xcd >> 1) * 8 + (loc & 7);
    const int et = (xcd & 1) * 4 + (loc >> 3);
    const int s0 = st * 64, e0 = et * 160;
    const u16* W = (et < 4) ? (Wre + (size_t)e0 * D_) : (Wim + (size_t)(e0 - D_) * D_);
    const int tid = threadIdx.x, lane = tid & 63;
    const int w = __builtin_amdgcn_readfirstlane(tid >> 6);
    const int r = lane & 15, q = lane >> 4;
    const unsigned smem_base = (unsigned)(size_t)(smem);

    const int xrow0 = (w < 4) ? w * 12 : 48 + (w - 4) * 4;
    u32x4 xv[3][5];
    {
        const unsigned xo = (unsigned)(((s0 + xrow0 + q) * D_ + r * 8) * 2);
#pragma unroll
        for (int c = 0; c < 5; ++c)
            asm volatile("global_load_dwordx4 %0, %1, %2" : "=v"(xv[0][c]) : "v"(xo + (unsigned)(c * 256)), "s"(x) : "memory");
        const unsigned xo2 = (w < 4) ? xo + (unsigned)(4 * D_ * 2) : xo;
        const unsigned st2 = (w < 4) ? (unsigned)(4 * D_ * 2) : 0u;
#pragma unroll
        for (int i = 1; i < 3; ++i)
#pragma unroll
            for (int c = 0; c < 5; ++c)
                asm volatile("global_load_dwordx4 %0, %1, %2" : "=v"(xv[i][c]) : "v"(xo2 + (unsigned)(i - 1) * st2 + (unsigned)(c * 256)), "s"(x) : "memory");
    }
    unsigned wv[5];
    const int p = w - 4;
#pragma unroll
    for (int i = 0; i < 5; ++i) {
        const int row = (p & 3) * 40 + i * 8 + (lane >> 3);
        wv[i] = (unsigned)(row * D_ * 2 + (((lane & 7) ^ (row & 7)) * 16));
    }
    const unsigned wl = smem_base + G1_XN + (unsigned)((p & 3) * 5120);
#define G1_ISSUE(K)                                                                     \
    do {                                                                                \
        _Pragma("unroll") for (int i_ = 0; i_ < 5; ++i_)                                \
            dma16(W + (K) * 64, wv[i_], wl + (unsigned)(((K) & 3) * G1_SLOT + i_ * 1024)); \
    } while (0)
    if (w >= 4) { G1_ISSUE(0); G1_ISSUE(1); G1_ISSUE(2); G1_ISSUE(3); }
    unsigned pf = 0;
    if (w < 4) {
        int li = w * 64 + lane;
        li = li < 200 ? li : 199;
        pf = l2_touch(W + (size_t)((loc & 7) * 20) * D_, (unsigned)((li % 20) * D_ * 2 + (li / 20) * 128));
    }

    if (w >= 4) asm volatile("s_waitcnt vmcnt(20)" ::: "memory");
    else asm volatile("s_waitcnt vmcnt(1)" ::: "memory");
    asm volatile("" : "+v"(xv[0][0]), "+v"(xv[0][1]), "+v"(xv[0][2]), "+v"(xv[0][3]), "+v"(xv[0][4]),
                      "+v"(xv[1][0]), "+v"(xv[1][1]), "+v"(xv[1][2]), "+v"(xv[1][3]), "+v"(xv[1][4]),
                      "+v"(xv[2][0]), "+v"(xv[2][1]), "+v"(xv[2][2]), "+v"(xv[2][3]), "+v"(xv[2][4]) :: "memory");
#pragma unroll
    for (int i = 0; i < 3; ++i) {
        if (i > 0 && w >= 4) break;
        f32x2 v[20];
        f32x2 s2 = {0.f, 0.f};
#pragma unroll
        for (int c = 0; c < 5; ++c)
#pragma unroll
            for (int j = 0; j < 4; ++j) { v[c * 4 + j] = unp2(xv[i][c][j]); s2 += v[c * 4 + j]; }
        const float mean = red16_sum(s2[0] + s2[1]) * (1.0f / D_);
        f32x2 q2 = {0.f, 0.f};
#pragma unroll
        for (int j = 0; j < 20; ++j) { const f32x2 d = v[j] - mean; q2 += d * d; }
        float m_bf, rinv;
        ln_finish(red16_sum(q2[0] + q2[1]), mean, m_bf, rinv);
        const int row = xrow0 + i * 4 + q;
#pragma unroll
        for (int c = 0; c < 5; ++c) {
            u32x4 o;
#pragma unroll
            for (int j = 0; j < 4; ++j) o[j] = cvt2(rbf2(v[c * 4 + j] - m_bf) * rinv);
            const int ch = (c * 16 + r) ^ (row & 15);
            *reinterpret_cast<u32x4*>(smem + row * 1280 + ch * 16) = o;
        }
    }

    if (w >= 4) {
        asm volatile("s_waitcnt vmcnt(15) lgkmcnt(0)\n\ts_barrier" ::: "memory");
#pragma unroll
        for (int k = 0; k < 10; ++k) {
            if (k <= 6) VM_BAR(10); else VM_BAR(0);
            if (k + 4 < 10) G1_ISSUE(k + 4);
        }
        return;
    }
    l2_touch_done1(pf);
    const int ws = w & 1, we = w >> 1;
    f32x4 acc[2][5];
#pragma unroll
    for (int m = 0; m < 2; ++m)
#pragma unroll
        for (int n = 0; n < 5; ++n) acc[m][n] = f32x4{0.f, 0.f, 0.f, 0.f};
    const char* xbase = smem + (ws * 32 + r) * 1280;
    const char* wbase = smem + G1_XN + (we * 80 + r) * 128;
    s16x8 xf[2][4], wf[2][10];
#define G1_READ(B, K)                                                                   \
    do {                                                                                \
        _Pragma("unroll") for (int h_ = 0; h_ < 2; ++h_) {                              \
            _Pragma("unroll") for (int m_ = 0; m_ < 2; ++m_)                            \
                xf[B][m_ * 2 + h_] = *reinterpret_cast<const s16x8*>(xbase + m_ * 16 * 1280 + ((((K) * 8 + h_ * 4 + q) ^ r) * 16)); \
            _Pragma("unroll") for (int n_ = 0; n_ < 5; ++n_)                            \
                wf[B][n_ * 2 + h_] = *reinterpret_cast<const s16x8*>(wbase + ((K) & 3) * G1_SLOT + n_ * 2048 + (((h_ * 4 + q) ^ (r & 7)) * 16)); \
        }                                                                               \
    } while (0)
    LGKM_BAR();
    G1_READ(0, 0);
#pragma unroll
    for (int k = 0; k < 10; ++k) {
        FRAG_BAR(xf[k & 1], wf[k & 1]);
        if (k + 1 < 10) G1_READ((k + 1) & 1, k + 1);
#pragma unroll
        for (int h = 0; h < 2; ++h)
#pragma unroll
            for (int m = 0; m < 2; ++m)
#pragma unroll
                for (int n = 0; n < 5; ++n)
                    acc[m][n] = __builtin_amdgcn_mfma_f32_16x16x32_bf16(xf[k & 1][m * 2 + h], wf[k & 1][n * 2 + h], acc[m][n], 0, 0, 0);
        if (k + 1 < 10) INTERLEAVE_14_20();
    }
    char* img = smem + G1_XN + 2 * G1_SLOT + w * 5120;
#pragma unroll
    for (int m = 0; m < 2; ++m)
#pragma unroll
        for (int n = 0; n < 5; ++n) {
            u32x2 o;
            o[0] = pack2(acc[m][n][0], acc[m][n][1]);
            o[1] = pack2(acc[m][n][2], acc[m][n][3]);
            *reinterpret_cast<u32x2*>(img + (n * 16 + r) * 64 + m * 32 + q * 8) = o;
        }
    u16* dst = abT + (size_t)(e0 + we * 80) * AP_ + s0 + ws * 32;
#pragma unroll
    for (int i = 0; i < 5; ++i) {
        const int c = i * 64 + lane;
        const u32x4 v = *reinterpret_cast<const u32x4*>(img + c * 16);
        *reinterpret_cast<u32x4*>(dst + (size_t)(c >> 2) * AP_ + (c & 3) * 8) = v;
    }
}

#define G2_T2 8
#define G2_T3 16
constexpr int G2_SLOT = (128 + 80) * 128;
constexpr int G2_NST = 6;
constexpr int G2_LDS = G2_NST * G2_SLOT;

__global__ __launch_bounds__(512, 2) void k_gemm2(const u16* __restrict__ Cre, const u16* __restrict__ Cim,
                                                  const u16* __restrict__ abT, u16* __restrict__ P,
                                                  const u16* __restrict__ wg, const u16* __restrict__ wu,
                                                  const u16* __restrict__ wd) {
    extern __shared__ __attribute__((aligned(16))) char smem[];
    const int bid = blockIdx.x, xcd = bid & 7, loc = bid >> 3;
    const int part = xcd & 1;
    const int tp = (xcd >> 1) * 4 + (loc >> 3);
    const int ct = loc & 7;
    const int t0 = tp * 128, e0 = ct * 80;
    const int tid = threadIdx.x, lane = tid & 63;
    const int w = __builtin_amdgcn_readfirstlane(tid >> 6);
    const int r = lane & 15, q = lane >> 4;
    const unsigned smem_base = (unsigned)(size_t)(smem);

    if (w >= 4) {
        __builtin_amdgcn_s_setprio(2);
        const int p = w - 4;
        const u16* Cb = (part ? Cim : Cre) + (size_t)t0 * S_;
        const u16* Bb = abT + (size_t)(part * D_ + e0) * AP_;
        unsigned av[4], bv[3];
#pragma unroll
        for (int i = 0; i < 4; ++i) {
            const int row = p * 32 + i * 8 + (lane >> 3);
            av[i] = (unsigned)(row * S_ * 2 + (((lane & 7) ^ (row & 7)) * 16));
        }
#pragma unroll
        for (int i = 0; i < 3; ++i) {
            const int o = p * 2560 + i * 1024 + lane * 16;
            const int row = o >> 7, cs = (o >> 4) & 7;
            bv[i] = (unsigned)(row * AP_ * 2 + ((cs ^ (row & 7)) * 16));
        }
        const unsigned la = smem_base + (unsigned)(p * 4096);
        const unsigned lb = smem_base + 16384u + (unsigned)(p * 2560);
#define G2_ISSUE(K, SO)                                                                 \
        do {                                                                            \
            const u16* cb_ = Cb + (K) * 64;                                             \
            const u16* bb_ = Bb + (K) * 64;                                             \
            _Pragma("unroll") for (int i_ = 0; i_ < 4; ++i_) dma16(cb_, av[i_], la + (SO) + i_ * 1024u); \
            dma16(bb_, bv[0], lb + (SO));                                               \
            dma16(bb_, bv[1], lb + (SO) + 1024u);                                       \
            if (lane < 32) dma16(bb_, bv[2], lb + (SO) + 2048u);                        \
        } while (0)
#pragma unroll
        for (int k = 0; k < G2_NST; ++k) G2_ISSUE(k, (unsigned)(k * G2_SLOT));
        VM_BAR(35);
        unsigned so = 0;
        for (int k = 0; k < 32; ++k) {
            if (k <= 26) VM_BAR(28); else VM_BAR(0);
            if (k + G2_NST < 32) G2_ISSUE(k + G2_NST, so);
            so = (so == (unsigned)((G2_NST - 1) * G2_SLOT)) ? 0u : so + (unsigned)G2_SLOT;
        }
        return;
    }
    const u16 *pfp0, *pfp1;
    {
        const int l0 = w * 9 + (lane >> 3), l1 = w * 9 + 8;
        const u16* Cp = (part ? Cim : Cre) + (size_t)(t0 + ct * 16) * S_ + (lane & 7) * 64;
        const u16* Bp = abT + (size_t)(part * D_ + e0 + (loc >> 3) * 20) * AP_ + (lane & 7) * 64;
        pfp0 = (l0 < 16) ? (Cp + (size_t)l0 * S_) : (Bp + (size_t)(l0 - 16) * AP_);
        pfp1 = (l1 < 16) ? (Cp + (size_t)l1 * S_) : (Bp + (size_t)(l1 - 16) * AP_);
    }
    unsigned pfd = 0;
#define G2_TOUCH(J)                                                                                     \
    do {                                                                                                \
        asm volatile("global_load_dword %0, %1, off" : "+v"(pfd) : "v"(pfp0 + (J) * 512) : "memory");   \
        asm volatile("global_load_dword %0, %1, off" : "+v"(pfd) : "v"(pfp1 + (J) * 512) : "memory");   \
    } while (0)
    G2_TOUCH(0); G2_TOUCH(1);
    f32x4 acc[5][2];
#pragma unroll
    for (int n = 0; n < 5; ++n)
#pragma unroll
        for (int m = 0; m < 2; ++m) acc[n][m] = f32x4{0.f, 0.f, 0.f, 0.f};
    const int rd0 = r * 128 + ((q ^ (r & 7)) * 16);
    const int rd1 = r * 128 + (((q + 4) ^ (r & 7)) * 16);
    const int aw = w * 4096;
    s16x8 cf[2][4], ef[2][10];
#define G2_READ(B, SO)                                                                  \
    do {                                                                                \
        const char* s0_ = smem + (SO) + rd0;                                            \
        const char* s1_ = smem + (SO) + rd1;                                            \
        _Pragma("unroll") for (int m_ = 0; m_ < 2; ++m_) {                              \
            cf[B][m_ * 2 + 0] = *reinterpret_cast<const s16x8*>(s0_ + aw + m_ * 2048);  \
            cf[B][m_ * 2 + 1] = *reinterpret_cast<const s16x8*>(s1_ + aw + m_ * 2048);  \
        }                                                                               \
        _Pragma("unroll") for (int n_ = 0; n_ < 5; ++n_) {                              \
            ef[B][n_ * 2 + 0] = *reinterpret_cast<const s16x8*>(s0_ + 16384 + n_ * 2048); \
            ef[B][n_ * 2 + 1] = *reinterpret_cast<const s16x8*>(s1_ + 16384 + n_ * 2048); \
        }                                                                               \
    } while (0)
#define G2_MMA(B)                                                                       \
    do {                                                                                \
        _Pragma("unroll") for (int h_ = 0; h_ < 2; ++h_)                                \
        _Pragma("unroll") for (int n_ = 0; n_ < 5; ++n_)                                \
        _Pragma("unroll") for (int m_ = 0; m_ < 2; ++m_)                                \
            acc[n_][m_] = __builtin_amdgcn_mfma_f32_16x16x32_bf16(ef[B][n_ * 2 + h_], cf[B][m_ * 2 + h_], acc[n_][m_], 0, 0, 0); \
    } while (0)
    LGKM_BAR();
    G2_READ(0, 0);
    int so = G2_SLOT;
    for (int k = 0; k < 32; k += 2) {
        FRAG_BAR(cf[0], ef[0]);
        if (k == G2_T2) G2_TOUCH(2);
        if (k == G2_T3) G2_TOUCH(3);
        if (k == 26) {
            const int li = (bid * 4 + w) * 64 + lane;
            const int mi = li / 15360;
            const u16* wp = (mi == 0 ? wg : (mi == 1 ? wu : wd)) + (size_t)(mi < 3 ? li - mi * 15360 : lane) * 64;
            asm volatile("global_load_dword %0, %1, off" : "+v"(pfd) : "v"(wp) : "memory");
        }
        G2_READ(1, so);
        so = (so == (G2_NST - 1) * G2_SLOT) ? 0 : so + G2_SLOT;
        G2_MMA(0);
        INTERLEAVE_14_20();
        FRAG_BAR(cf[1], ef[1]);
        G2_READ(0, so);
        so = (so == (G2_NST - 1) * G2_SLOT) ? 0 : so + G2_SLOT;
        G2_MMA(1);
        INTERLEAVE_14_20();
    }
    l2_touch_done1(pfd);
    char* img = smem + 3 * G2_SLOT + w * 5120;
#pragma unroll
    for (int m = 0; m < 2; ++m)
#pragma unroll
        for (int n = 0; n < 5; ++n) {
            u32x2 o;
            o[0] = pack2(acc[n][m][0], acc[n][m][1]);
            o[1] = pack2(acc[n][m][2], acc[n][m][3]);
            *reinterpret_cast<u32x2*>(img + (m * 16 + r) * 160 + n * 32 + q * 8) = o;
        }
    u16* dst = P + (size_t)part * S_ * D_ + (size_t)(t0 + w * 32) * D_ + e0;
#pragma unroll
    for (int i = 0; i < 5; ++i) {
        const int c = i * 64 + lane;
        const u32x4 v = *reinterpret_cast<const u32x4*>(img + c * 16);
        const int row = c / 10;
        *reinterpret_cast<u32x4*>(dst + (size_t)row * D_ + (c - row * 10) * 8) = v;
    }
}

__device__ __forceinline__ unsigned topk_key(float v, int idx) {
    unsigned b = __float_as_uint(v) >> 16;
    b = (b & 0x8000u) ? (~b & 0xffffu) : (b | 0x8000u);
    return (b << 11) | (unsigned)(S_ - 1 - idx);
}
__device__ __forceinline__ unsigned umax_(unsigned a, unsigned b) { return a > b ? a : b; }
__device__ __forceinline__ unsigned wave_max_u32(unsigned v) {
    v = umax_(v, dpp_u<0x128>(v)); v = umax_(v, dpp_u<0x124>(v)); v = umax_(v, dpp_u<0x122>(v)); v = umax_(v, dpp_u<0x121>(v));
    v = umax_(v, (unsigned)__builtin_amdgcn_update_dpp(0, (int)v, 0x142, 0xa, 0xf, false));
    v = umax_(v, (unsigned)__builtin_amdgcn_update_dpp(0, (int)v, 0x143, 0xc, 0xf, false));
    return __builtin_amdgcn_readlane(v, 63);
}
typedef __attribute__((ext_vector_type(4))) short s16x4;
typedef __attribute__((address_space(3))) s16x4* lds_s16x4_p;
__device__ __forceinline__ s16x8 tr_frag(const char* img, int lane) {
    const int i = lane & 15, g = lane >> 4;
    const char* a = img + (g * 8 + (i >> 2)) * 32 + (i & 3) * 8;
    const s16x4 lo = __builtin_amdgcn_ds_read_tr16_b64_v4i16((lds_s16x4_p)(a));
    const s16x4 hi = __builtin_amdgcn_ds_read_tr16_b64_v4i16((lds_s16x4_p)(a + 128));
    return s16x8{lo[0], lo[1], lo[2], lo[3], hi[0], hi[1], hi[2], hi[3]};
}

constexpr int CNT_STRIDE = 64;
constexpr unsigned RG_TAG = 0x5AA5u;
constexpr unsigned ACT_TAG = 0x7C3E91B5u;
constexpr unsigned SPIN_CAP = 1u << 20;
__device__ __forceinline__ void st_sc1_b128(void* p, u32x4 v) {
    asm volatile("global_store_dwordx4 %0, %1, off sc1\n\ts_nop 1" :: "v"(p), "v"(v) : "memory");
}
__device__ __forceinline__ void st_sc1_b64(void* p, u32x2 v) {
    asm volatile("global_store_dwordx2 %0, %1, off sc1\n\ts_nop 1" :: "v"(p), "v"(v) : "memory");
}
__device__ __forceinline__ void st_sc1_b32(void* p, unsigned v) {
    asm volatile("global_store_dword %0, %1, off sc1\n\ts_nop 1" :: "v"(p), "v"(v) : "memory");
}

__device__ __forceinline__ int route_top8(const u32x2* rg, unsigned* cand, u32x2* lrg, int w, int lane, u32x2& pay) {
    u32x4 g[4];
    for (unsigned spins = 0; spins < SPIN_CAP; ++spins) {
#pragma unroll
        for (int i = 0; i < 4; ++i)
            asm volatile("global_load_dwordx4 %0, %1, %2 sc1" : "=v"(g[i]) : "v"((unsigned)((w * 256 + i * 64 + lane) * 16)), "s"(rg) : "memory");
        asm volatile("s_waitcnt vmcnt(0)" : "+v"(g[0]), "+v"(g[1]), "+v"(g[2]), "+v"(g[3]) :: "memory");
        bool ok = true;
#pragma unroll
        for (int i = 0; i < 4; ++i) ok = ok && ((g[i][1] >> 16) == RG_TAG) && ((g[i][3] >> 16) == RG_TAG);
        if (__all(ok)) break;
        __builtin_amdgcn_s_sleep(2);
    }
    unsigned key[8];
#pragma unroll
    for (int i = 0; i < 4; ++i) {
        const int idx = (w * 256 + i * 64 + lane) * 2;
        key[2 * i] = topk_key(lo_f(g[i][0]), idx);
        key[2 * i + 1] = topk_key(lo_f(g[i][2]), idx + 1);
        *reinterpret_cast<u32x4*>(lrg + idx) = g[i];
    }
    unsigned prev = 1u << 27, my_key = 0;
#pragma unroll
    for (int t = 0; t < 8; ++t) {
        unsigned m = 0;
#pragma unroll
        for (int i = 0; i < 8; ++i) m = umax_(m, key[i] - prev);
        prev += wave_max_u32(m);
        my_key = (lane == t) ? prev : my_key;
    }
    if (lane < 8) cand[w * 8 + lane] = my_key;
    __syncthreads();
    const unsigned ck = cand[lane & 31];
    prev = 1u << 27; my_key = 0;
#pragma unroll
    for (int t = 0; t < 8; ++t) {
        prev += wave_max_u32(ck - prev);
        my_key = ((lane & 7) == t) ? prev : my_key;
    }
    const int row = (S_ - 1 - (int)(my_key & 2047u)) & 2047;
    pay = lrg[row];
    return row;
}

__global__ __launch_bounds__(256, 1) void k_tail(const u16* __restrict__ x, const u16* __restrict__ P,
                                              const u16* __restrict__ wr, const u16* __restrict__ wg,
                                              const u16* __restrict__ wu, const u16* __restrict__ wd,
                                              u16* out, u32x2* rg, u32x2* actg, unsigned* cnt) {
    __shared__ __attribute__((aligned(16))) char wimg[48 * 1024];
    __shared__ __attribute__((aligned(16))) u32x2 lrg[2048];
    __shared__ __attribute__((aligned(16))) char tok[4 * 8 * 784];
    __shared__ float red[4][2][8][16];
    __shared__ unsigned cand[32];
    __shared__ unsigned arrived;
    const int bid = blockIdx.x;
    const int tid = threadIdx.x, lane = tid & 63, r = lane & 15, q = lane >> 4;
    const int w = __builtin_amdgcn_readfirstlane(tid >> 6);
    const unsigned l0 = (unsigned)(size_t)(&wimg[0]);

    const bool up_role = bid >= 128;
    if (!up_role) {
        if (tid == 0) arrived = 0u;
        __syncthreads();
        {
            const int row = bid * 16 + w * 4 + q;
            const size_t ro = (size_t)row * D_;
            u32x4 xv[5], p1[5], p2[5], wv[5];
    #pragma unroll
            for (int c = 0; c < 5; ++c) {
                const int o = (c * 16 + r) * 8;
                xv[c] = *reinterpret_cast<const u32x4*>(x + ro + o);
                p1[c] = *reinterpret_cast<const u32x4*>(P + ro + o);
                p2[c] = *reinterpret_cast<const u32x4*>(P + (size_t)S_ * D_ + ro + o);
                wv[c] = *reinterpret_cast<const u32x4*>(wr + o);
            }
            f32x2 h[20];
            f32x2 s2 = {0.f, 0.f};
    #pragma unroll
            for (int c = 0; c < 5; ++c)
    #pragma unroll
                for (int j = 0; j < 4; ++j) {
                    h[c * 4 + j] = rbf2(unp2(xv[c][j]) + rbf2(unp2(p1[c][j]) - unp2(p2[c][j])));
                    s2 += h[c * 4 + j];
                }
            const float mean = red16_sum(s2[0] + s2[1]) * (1.0f / D_);
            f32x2 q2 = {0.f, 0.f};
    #pragma unroll
            for (int i = 0; i < 20; ++i) { const f32x2 d = h[i] - mean; q2 += d * d; }
            const float sq = red16_sum(q2[0] + q2[1]);
            const float m_bf = rbf(mean);
            const float den = rbf(sqrtf(rbf(rbf(sq * (1.0f / D_)) + 1e-5f)));
            const float rinv = 1.0f / den;
            f32x2 d2 = {0.f, 0.f};
            u32x4 o[5];
    #pragma unroll
            for (int c = 0; c < 5; ++c)
    #pragma unroll
                for (int j = 0; j < 4; ++j) {
                    const f32x2 a = rbf2(rbf2(h[c * 4 + j] - m_bf) * rinv);
                    d2 += a * unp2(wv[c][j]);
                    o[c][j] = cvt2(h[c * 4 + j] + a);
                }
            const float dot = red16_sum(d2[0] + d2[1]);
            if (r == 0) {
                u32x2 gv; gv[0] = pack2(dot, m_bf); gv[1] = (unsigned)f2bf(den) | (RG_TAG << 16);
                st_sc1_b64(rg + row, gv);
            }
    #pragma unroll
            for (int c = 0; c < 5; ++c) st_sc1_b128(out + ro + (c * 16 + r) * 8, o[c]);
            asm volatile("s_waitcnt vmcnt(0)" ::: "memory");
            if (lane == 0) {
                const unsigned old = __hip_atomic_fetch_add(&arrived, 1u, __ATOMIC_RELAXED, __HIP_MEMORY_SCOPE_WORKGROUP);
                if (old == 3u) __hip_atomic_fetch_add(cnt + (bid & 7) * CNT_STRIDE, 1u, __ATOMIC_RELAXED, __HIP_MEMORY_SCOPE_AGENT);
            }
        }
    if (bid >= 40) return;
    }
    const int ju = bid - 128;
    const int c0 = up_role ? ((ju & 7) * 12 + (ju >> 3)) * 16 : ((bid & 7) * 5 + (bid >> 3)) * 16;
    if (up_role) {
        const unsigned voff = (unsigned)(((w * 160 + (lane >> 1)) * F_ + c0 + (lane & 1) * 8) * 2);
#pragma unroll
        for (int i = 0; i < 5; ++i) {
            dma16(wg, voff + (unsigned)(i * 32 * F_ * 2), l0 + (unsigned)((w * 5 + i) * 1024));
            dma16(wu, voff + (unsigned)(i * 32 * F_ * 2), l0 + (unsigned)(20480 + (w * 5 + i) * 1024));
        }
    } else {
        const unsigned voff = (unsigned)(((w * 384 + (lane >> 1)) * D_ + c0 + (lane & 1) * 8) * 2);
#pragma unroll
        for (int i = 0; i < 12; ++i) dma16(wd, voff + (unsigned)(i * 32 * D_ * 2), l0 + (unsigned)((w * 12 + i) * 1024));
    }
    u32x2 pay;
    const int my_row = route_top8(rg, cand, lrg, w, lane, pay);

    if (up_role) {
        {
            u32x4 xv[3], p1[3], p2[3];
            int tk[3], ch[3];
#pragma unroll
            for (int i = 0; i < 3; ++i) {
                int c = i * 64 + lane; c = c < 160 ? c : 159;
                tk[i] = 2 * w + (c >= 80 ? 1 : 0); ch[i] = c >= 80 ? c - 80 : c;
                const int rw = __shfl(my_row, tk[i]);
                const size_t o = (size_t)rw * D_ + ch[i] * 8;
                xv[i] = *reinterpret_cast<const u32x4*>(x + o);
                p1[i] = *reinterpret_cast<const u32x4*>(P + o);
                p2[i] = *reinterpret_cast<const u32x4*>(P + (size_t)S_ * D_ + o);
            }
#pragma unroll
            for (int i = 0; i < 3; ++i) {
                const float sm = hi_f(__shfl(pay[0], tk[i])), sr = 1.0f / lo_f(__shfl(pay[1], tk[i]));
                u32x4 lp;
#pragma unroll
                for (int j = 0; j < 4; ++j) {
                    const f32x2 hh = rbf2(unp2(xv[i][j]) + rbf2(unp2(p1[i][j]) - unp2(p2[i][j])));
                    lp[j] = cvt2(rbf2(hh - sm) * sr);
                }
                *reinterpret_cast<u32x4*>(tok + tk[i] * 1296 + ch[i] * 16) = lp;
            }
        }
        __syncthreads();
        f32x4 ag = {0.f, 0.f, 0.f, 0.f}, au = {0.f, 0.f, 0.f, 0.f};
#pragma unroll
        for (int i = 0; i < 5; ++i) {
            const s16x8 af = *reinterpret_cast<const s16x8*>(tok + (r & 7) * 1296 + ((w * 5 + i) * 4 + q) * 16);
            ag = __builtin_amdgcn_mfma_f32_16x16x32_bf16(af, tr_frag(&wimg[(w * 5 + i) * 1024], lane), ag, 0, 0, 0);
            au = __builtin_amdgcn_mfma_f32_16x16x32_bf16(af, tr_frag(&wimg[20480 + (w * 5 + i) * 1024], lane), au, 0, 0, 0);
        }
        if (q < 2) {
#pragma unroll
            for (int j = 0; j < 4; ++j) { red[w][0][q * 4 + j][r] = ag[j]; red[w][1][q * 4 + j][r] = au[j]; }
        }
        __syncthreads();
        if (tid < 64) {
            const int t = tid >> 3, c = (tid & 7) * 2;
            float o2[2];
#pragma unroll
            for (int e = 0; e < 2; ++e) {
                float g = red[0][0][t][c + e] + red[1][0][t][c + e] + red[2][0][t][c + e] + red[3][0][t][c + e];
                float u = red[0][1][t][c + e] + red[1][1][t][c + e] + red[2][1][t][c + e] + red[3][1][t][c + e];
                g = rbf(g); u = rbf(u);
                o2[e] = rbf(g / (1.0f + __expf(-g))) * u;
            }
            u32x2 gv; gv[0] = pack2(o2[0], o2[1]); gv[1] = ACT_TAG;
            st_sc1_b64(actg + t * (F_ / 2) + (c0 + c) / 2, gv);
        }
        return;
    }

    float hv[2], lv[2], sg;
    {
        const size_t o = (size_t)my_row * D_ + c0 + (lane >> 3) * 2;
        const unsigned xv = *reinterpret_cast<const unsigned*>(x + o);
        const unsigned p1 = *reinterpret_cast<const unsigned*>(P + o);
        const unsigned p2 = *reinterpret_cast<const unsigned*>(P + (size_t)S_ * D_ + o);
        const float sm = hi_f(pay[0]), sr = 1.0f / lo_f(pay[1]);
        hv[0] = rbf(lo_f(xv) + rbf(lo_f(p1) - lo_f(p2)));
        hv[1] = rbf(hi_f(xv) + rbf(hi_f(p1) - hi_f(p2)));
        lv[0] = rbf(rbf(hv[0] - sm) * sr); lv[1] = rbf(rbf(hv[1] - sm) * sr);
        sg = rbf(1.0f / (1.0f + __expf(-lo_f(pay[0]))));
    }
    if (w == 0) {
        for (unsigned spins = 0; spins < SPIN_CAP; ++spins) {
            unsigned v = 0;
            if (lane < 8) v = __hip_atomic_load(cnt + lane * CNT_STRIDE, __ATOMIC_RELAXED, __HIP_MEMORY_SCOPE_AGENT);
            v += dpp_u<0x0B1>(v); v += dpp_u<0x04E>(v); v += dpp_u<0x141>(v);
            if (__builtin_amdgcn_readfirstlane(v) >= 128u) break;
            __builtin_amdgcn_s_sleep(2);
        }
    }
    for (unsigned spins = 0; spins < SPIN_CAP; ++spins) {
        u32x2 g1;
        asm volatile("global_load_dwordx2 %0, %1, off sc1\n\ts_waitcnt vmcnt(0)" : "=v"(g1)
                     : "v"(actg + 7 * (F_ / 2) + w * 192 + (lane < 24 ? lane : 23) * 8 + 7) : "memory");
        if (__all(g1[1] == ACT_TAG)) break;
        __builtin_amdgcn_s_sleep(1);
    }
    u32x4 ga[12];
    for (unsigned spins = 0; spins < SPIN_CAP; ++spins) {
#pragma unroll
        for (int i = 0; i < 12; ++i) {
            const int c = i * 64 + lane, t = c / 96;
            asm volatile("global_load_dwordx4 %0, %1, off sc1" : "=v"(ga[i]) : "v"(actg + t * (F_ / 2) + w * 192 + (c - t * 96) * 2) : "memory");
        }
        asm volatile("s_waitcnt vmcnt(0)"
                     : "+v"(ga[0]), "+v"(ga[1]), "+v"(ga[2]), "+v"(ga[3]), "+v"(ga[4]), "+v"(ga[5]),
                       "+v"(ga[6]), "+v"(ga[7]), "+v"(ga[8]), "+v"(ga[9]), "+v"(ga[10]), "+v"(ga[11]) :: "memory");
        bool ok = true;
#pragma unroll
        for (int i = 0; i < 12; ++i) ok = ok && (ga[i][1] == ACT_TAG) && (ga[i][3] == ACT_TAG);
        if (__all(ok)) break;
        __builtin_amdgcn_s_sleep(1);
    }
    char* aimg = tok + w * (8 * 784);
#pragma unroll
    for (int i = 0; i < 12; ++i) {
        const int c = i * 64 + lane, t = c / 96;
        u32x2 v; v[0] = ga[i][0]; v[1] = ga[i][2];
        *reinterpret_cast<u32x2*>(aimg + t * 784 + (c - t * 96) * 8) = v;
    }
    f32x4 a = {0.f, 0.f, 0.f, 0.f};
#pragma unroll
    for (int i = 0; i < 12; ++i) {
        const s16x8 af = *reinterpret_cast<const s16x8*>(aimg + (r & 7) * 784 + (i * 4 + q) * 16);
        a = __builtin_amdgcn_mfma_f32_16x16x32_bf16(af, tr_frag(&wimg[(w * 12 + i) * 1024], lane), a, 0, 0, 0);
    }
    if (q < 2) {
#pragma unroll
        for (int j = 0; j < 4; ++j) red[w][0][q * 4 + j][r] = a[j];
    }
    __syncthreads();
    if (w == 0) {
        const int et = lane & 7, ec = (lane >> 3) * 2;
        float o2[2];
#pragma unroll
        for (int e = 0; e < 2; ++e) {
            float y = red[0][0][et][ec + e] + red[1][0][et][ec + e] + red[2][0][et][ec + e] + red[3][0][et][ec + e];
            y = rbf(rbf(y) * sg);
            o2[e] = hv[e] + rbf(lv[e] + y);
        }
        st_sc1_b32(out + (size_t)my_row * D_ + c0 + ec, pack2(o2[0], o2[1]));
    }
}

extern "C" void kernel_launch(void* const* d_in, const int* in_sizes, int n_in,
                              void* d_out, int out_size, void* d_ws, size_t ws_size,
                              hipStream_t stream) {
    const u16* x   = (const u16*)d_in[0];
    const u16* Cre = (const u16*)d_in[1];
    const u16* Cim = (const u16*)d_in[2];
    const u16* Wre = (const u16*)d_in[3];
    const u16* Wim = (const u16*)d_in[4];
    const u16* wr  = (const u16*)d_in[6];
    const u16* wg  = (const u16*)d_in[7];
    const u16* wu  = (const u16*)d_in[8];
    const u16* wd  = (const u16*)d_in[9];
    u16* out = (u16*)d_out;

    char* ws = (char*)d_ws;
    u16* abT = (u16*)ws;
    u16* P = (u16*)(ws + (size_t)2 * D_ * AP_ * 2);
    char* tail = ws + (size_t)2 * D_ * AP_ * 2 + (size_t)2 * D_ * S_ * 2;
    u32x2* rg = (u32x2*)tail;
    u32x2* actg = (u32x2*)(tail + 16384);
    unsigned* cnt = (unsigned*)(tail + 16384 + 49152);

    (void)hipFuncSetAttribute((const void*)k_gemm1, hipFuncAttributeMaxDynamicSharedMemorySize, G1_LDS);
    (void)hipFuncSetAttribute((const void*)k_gemm2, hipFuncAttributeMaxDynamicSharedMemorySize, G2_LDS);

    k_gemm1<<<256, 512, G1_LDS, stream>>>(x, Wre, Wim, abT, (u32x4*)tail);
    k_gemm2<<<256, 512, G2_LDS, stream>>>(Cre, Cim, abT, P, wg, wu, wd);
    k_tail<<<224, 256, 0, stream>>>(x, P, wr, wg, wu, wd, out, rg, actg, cnt);
}
```
